```python
import torch

def _softmax(x, axis=-1):
    return torch.softmax(x, dim=axis)
def _gelu(x):
    return torch.nn.functional.gelu(x, approximate="tanh")
def _silu(x):
    return torch.nn.functional.silu(x)
def _sigmoid(x):
    return torch.sigmoid(x)
def _layernorm(x, axis=-1, eps=1e-5):
    m = x.mean(dim=axis, keepdim=True)
    v = x.var(dim=axis, keepdim=True, unbiased=False)
    return (x - m) / torch.sqrt(v + eps)
def _scatter_add(tgt, idx, src, axis=0):
    return tgt.clone().index_add_(axis, idx, src)
def _segment_sum(src, seg, n):
    out = torch.zeros((n,) + src.shape[1:], dtype=src.dtype)
    return out.index_add_(0, seg, src)
def _pad(x, pw):
    flat = tuple(v for pair in reversed(pw) for v in pair)
    return torch.nn.functional.pad(x, flat)

kernel_name = 'gated_mix__softmax_full_mha__dense_activation_squared_relu__prompt'

def setup_inputs():

    _w = (lambda a: (lambda t: t.to(torch.bfloat16) if t.is_floating_point() else t)(torch.as_tensor(a)))
    def _A_setup():
        B, S, D = 1, 2048, 640
        return {
            "x": _w((lambda *s: torch.randn(*s, dtype=torch.bfloat16) * 0.05)(B, S, D)),
            "wqkv": _w((lambda *s: torch.randn(*s, dtype=torch.bfloat16) * 0.8)(D, 3 * D)),
            "wo": _w((lambda *s: torch.randn(*s, dtype=torch.bfloat16) * 0.1)(D, D)),
        }
    _A_in = _A_setup()

    def _B_setup():
        B, S, D, F = 1, 2048, 640, 1536
        return {
            "x": _w((lambda *s: torch.randn(*s, dtype=torch.bfloat16) * 0.05)(B, S, D)),
            "w1": _w((lambda *s: torch.randn(*s, dtype=torch.bfloat16) * 0.1)(D, F)),
            "b1": _w((lambda *s: torch.randn(*s, dtype=torch.bfloat16) * 0.1)(F)),
            "w2": _w((lambda *s: torch.randn(*s, dtype=torch.bfloat16) * 0.1)(F, D)),
            "b2": _w((lambda *s: torch.randn(*s, dtype=torch.bfloat16) * 0.05)(D)),
        }
    _B_in = _B_setup()

    return {
        "A_x": _A_in["x"],
        "A_wqkv": _A_in["wqkv"],
        "A_wo": _A_in["wo"],
        "B_x": _B_in["x"],
        "B_w1": _B_in["w1"],
        "B_b1": _B_in["b1"],
        "B_w2": _B_in["w2"],
        "B_b2": _B_in["b2"],
        "gate_w": _w((lambda *s: torch.randn(*s, dtype=torch.bfloat16) * 0.05)(640) * 16.0),
    }

def reference(A_x, A_wqkv, A_wo, B_x, B_w1, B_b1, B_w2, B_b2, gate_w):
    def _A_ref(x, wqkv, wo):
        B, S, D = x.shape
        H = 32; Dh = D // H
        qkv = (lambda x, *d: x.permute(*d))((x @ wqkv).reshape(B, S, 3, H, Dh), 2, 0, 3, 1, 4)
        q, k, v = qkv[0], qkv[1], qkv[2]
        scale = Dh ** -0.5
        scores = torch.einsum("bhqd,bhkd->bhqk", q, k) * scale
        p = _softmax(scores, axis=-1)
        o = torch.einsum("bhqk,bhkd->bhqd", p, v)
        return (lambda x, *d: x.permute(*d))(o, 0, 2, 1, 3).reshape(B, S, D) @ wo

    def _B_ref(x, w1, b1, w2, b2):
        h = torch.einsum("bsd,df->bsf", x, w1) + b1
        zero = torch.zeros_like(h)
        r = torch.where(h > zero, h, zero)
        h = r * r
        out = torch.einsum("bsf,fd->bsd", h, w2) + b2
        return out

    x = A_x
    g = _sigmoid((x * gate_w).sum(-1, keepdims=True))
    a = _A_ref(x, A_wqkv, A_wo)
    b = _B_ref(x, B_w1, B_b1, B_w2, B_b2)
    return g * a + (1 - g) * b
```

```cpp
#include <hip/hip_runtime.h>
#include <hip/hip_bf16.h>

typedef unsigned short u16;
typedef unsigned int u32;
typedef __attribute__((ext_vector_type(8))) __bf16 bf16x8;
typedef __attribute__((ext_vector_type(2))) __bf16 bf16x2;
typedef __attribute__((ext_vector_type(4))) short s16x4;
typedef __attribute__((ext_vector_type(16))) float f32x16;
typedef __attribute__((ext_vector_type(2))) float f32x2;
typedef __attribute__((ext_vector_type(4))) float f32x4;
typedef __attribute__((ext_vector_type(4))) u32 u32x4;
typedef __attribute__((ext_vector_type(2))) u32 u32x2;
typedef __amdgpu_buffer_rsrc_t rsrc_t;
#define LDSP(T) __attribute__((address_space(3))) T*

constexpr int SEQ = 2048, DM = 640, FF = 1536, NH = 32;
constexpr u32 KV_TILE = 3072, Q_TILE = 1536, AH_STEPS = 136;
constexpr size_t WS_KV = 0;
constexpr size_t WS_Q = WS_KV + (size_t)NH * 64 * KV_TILE;
constexpr size_t WS_AH = WS_Q + (size_t)NH * 64 * Q_TILE;
constexpr size_t WS_G = WS_AH + (size_t)64 * AH_STEPS * 1024;
constexpr size_t WS_BP = WS_G + SEQ * 4;
constexpr float QSCALE = 0.22360679774997896f * 1.4426950408889634f;

#define MFMA(a, b, c) __builtin_amdgcn_mfma_f32_32x32x16_bf16((a), (b), (c), 0, 0, 0)
#define WAIT_VM(n) asm volatile("s_waitcnt vmcnt(" #n ")" ::: "memory")
#define WAIT_LGKM0() asm volatile("s_waitcnt lgkmcnt(0)" ::: "memory")

__device__ __forceinline__ rsrc_t mkrsrc(const void* p, u32 bytes) {
  return __builtin_amdgcn_make_buffer_rsrc((void*)p, 0, (int)bytes, 0x00020000);
}
__device__ __forceinline__ void dma16(rsrc_t r, u32 lds, u32 voff, u32 soff) {
  asm volatile("s_mov_b32 m0, %0\n\ts_nop 0\n\tbuffer_load_dwordx4 %1, %2, %3 offen lds"
               :: "s"(lds), "v"(voff), "s"(r), "s"(soff) : "m0", "memory");
}
template <int N> __device__ __forceinline__ void wait_vm() {
  asm volatile("s_waitcnt vmcnt(%0)" :: "n"(N) : "memory");
}
__device__ __forceinline__ u32 rfl(u32 v) { return __builtin_amdgcn_readfirstlane(v); }
__device__ __forceinline__ u32 pk2(float a, float b) {
  f32x2 v = {a, b};
  return __builtin_bit_cast(u32, __builtin_convertvector(v, bf16x2));
}
__device__ __forceinline__ float bflo(u32 v) { return __uint_as_float(v << 16); }
__device__ __forceinline__ float bfhi(u32 v) { return __uint_as_float(v & 0xffff0000u); }
__device__ __forceinline__ bf16x8 lds128(u32 a) { return *(LDSP(bf16x8))a; }
__device__ __forceinline__ bf16x8 ldstr(u32 a0, u32 a1) {
  s16x4 lo = __builtin_amdgcn_ds_read_tr16_b64_v4i16((LDSP(s16x4))a0);
  s16x4 hi = __builtin_amdgcn_ds_read_tr16_b64_v4i16((LDSP(s16x4))a1);
  return __builtin_bit_cast(bf16x8, __builtin_shufflevector(lo, hi, 0, 1, 2, 3, 4, 5, 6, 7));
}
__device__ __forceinline__ bf16x8 pack8(const f32x16& a, int s) {
  u32x4 r = {pk2(a[8 * s], a[8 * s + 1]), pk2(a[8 * s + 2], a[8 * s + 3]),
             pk2(a[8 * s + 4], a[8 * s + 5]), pk2(a[8 * s + 6], a[8 * s + 7])};
  return __builtin_bit_cast(bf16x8, r);
}

constexpr int K1_NS = 2;

template <int MODE>
__device__ __forceinline__ void k1_tile(const u16* x, const u16* w, u32 ldw, u32 wbytes, int rb, int col0,
                                        char* ws, const u16* b1, u32 sm) {
  const int tid = threadIdx.x, lane = tid & 63, wid = rfl(tid >> 6), hf = lane >> 5, l32 = lane & 31;
  const int wm = wid >> 1, wn = wid & 1;
  rsrc_t rx = mkrsrc(x, SEQ * DM * 2), rw = mkrsrc(w, wbytes);
  u32 xoff[4], woff[4];
#pragma unroll
  for (int i = 0; i < 4; ++i) {
    int row = (wid * 4 + i) * 8 + (lane >> 3);
    int c = (lane & 7) ^ ((row >> 1) & 7);
    xoff[i] = (rb * 128 + row) * (DM * 2) + c * 16;
    int krow = (wid * 4 + i) * 4 + ((lane >> 2) & 3);
    woff[i] = krow * ldw * 2 + col0 * 2 + (lane >> 4) * 64 + (lane & 3) * 16;
  }
  auto issue = [&](int kt) {
    u32 base = sm + (kt % K1_NS) * 32768 + wid * 4096;
#pragma unroll
    for (int i = 0; i < 4; ++i) dma16(rx, base + i * 1024, xoff[i], kt * 128);
#pragma unroll
    for (int i = 0; i < 4; ++i) dma16(rw, base + 16384 + i * 1024, woff[i], kt * 64 * ldw * 2);
  };
  f32x16 acc[2][2] = {};
  u32 xa[2][2];
#pragma unroll
  for (int m = 0; m < 2; ++m) {
    int row = wm * 64 + m * 32 + l32;
    xa[m][0] = sm + row * 128 + ((hf ^ ((row >> 1) & 7)) << 4);
    xa[m][1] = sm + row * 128 + (((2 + hf) ^ ((row >> 1) & 7)) << 4);
  }
  const u32 wa = sm + 16384 + hf * 2048 + wn * 512 + ((lane >> 2) & 3) * 64 + ((lane >> 4) & 1) * 32 + (lane & 3) * 8;
#pragma unroll
  for (int s = 0; s < K1_NS - 1; ++s) issue(s);
#pragma unroll
  for (int kt = 0; kt < 10; ++kt) {
    if (kt + K1_NS - 2 < 10) wait_vm<8 * (K1_NS - 2)>(); else wait_vm<0>();
    __builtin_amdgcn_s_barrier();
    if (kt + K1_NS - 1 < 10) issue(kt + K1_NS - 1);
    const u32 so = (kt % K1_NS) * 32768;
#pragma unroll
    for (int ks = 0; ks < 4; ++ks) {
      bf16x8 xf[2], wf[2];
#pragma unroll
      for (int m = 0; m < 2; ++m) xf[m] = lds128((xa[m][ks & 1] ^ ((ks >> 1) << 6)) + so);
#pragma unroll
      for (int n = 0; n < 2; ++n) wf[n] = ldstr(wa + so + ks * 4096 + n * 256, wa + so + ks * 4096 + 1024 + n * 256);
#pragma unroll
      for (int m = 0; m < 2; ++m)
#pragma unroll
        for (int n = 0; n < 2; ++n)
          acc[m][n] = (MODE == 1) ? MFMA(xf[m], wf[n], acc[m][n]) : MFMA(wf[n], xf[m], acc[m][n]);
    }
  }
#pragma unroll
  for (int m = 0; m < 2; ++m) {
    const int tt = rb * 4 + wm * 2 + m;
#pragma unroll
    for (int n = 0; n < 2; ++n) {
      const int f0 = col0 + wn * 64 + n * 32;
      const f32x16& a = acc[m][n];
      if (MODE == 0) {
        const bool isq = f0 < DM;
        const float sc = isq ? QSCALE : 1.0f;
#pragma unroll
        for (int g = 0; g < 4; ++g) {
          int fb = f0 - (isq ? 0 : DM) + 8 * g + 4 * hf;
          int h = fb / 20, d = fb - h * 20;
          char* t = isq ? ws + WS_Q + (size_t)(h * 64 + tt) * Q_TILE : ws + WS_KV + (size_t)(h * 64 + tt) * KV_TILE;
          u32 v0 = pk2(a[4 * g] * sc, a[4 * g + 1] * sc), v1 = pk2(a[4 * g + 2] * sc, a[4 * g + 3] * sc);
          if (d < 16) {
            *(u32x2*)(t + (d >> 3) * 512 + l32 * 16 + (d & 4) * 2) = u32x2{v0, v1};
          } else {
            *(u32x4*)(t + 1024 + l32 * 16) = u32x4{v0, v1, 0x3f803f80u, 0x00003f80u};
          }
        }
      } else if (MODE == 1) {
        int c = f0 - 2 * DM + l32;
        int h = c / 20, d = c - h * 20;
        char* t = ws + WS_KV + (size_t)(h * 64 + tt) * KV_TILE + 1536 + hf * 384;
#pragma unroll
        for (int s = 0; s < 2; ++s) {
          *(bf16x8*)(t + s * 768 + d * 16) = pack8(a, s);
          if (d == 19) *(u32x4*)(t + s * 768 + 20 * 16) = u32x4{0x3f803f80u, 0x3f803f80u, 0x3f803f80u, 0x3f803f80u};
        }
      } else {
        f32x16 r;
#pragma unroll
        for (int g = 0; g < 4; ++g) {
          u32x2 bb = *(const u32x2*)(b1 + f0 + 8 * g + 4 * hf);
          float bv[4] = {bflo(bb[0]), bfhi(bb[0]), bflo(bb[1]), bfhi(bb[1])};
#pragma unroll
          for (int i = 0; i < 4; ++i) {
            float v = fmaxf(a[4 * g + i] + bv[i], 0.f);
            r[4 * g + i] = v * v;
          }
        }
#pragma unroll
        for (int s = 0; s < 2; ++s)
          *(bf16x8*)(ws + WS_AH + ((size_t)(tt * AH_STEPS + 40 + (f0 >> 4) + s) * 64 + lane) * 16) = pack8(r, s);
      }
    }
  }
}

__global__ __launch_bounds__(256) void k1_proj(const u16* x, const u16* wqkv, const u16* w1, const u16* b1,
                                               const u16* gw, char* ws) {
  __shared__ __attribute__((aligned(1024))) char smem[K1_NS * 32768];
  const u32 sm = (u32)(size_t)(LDSP(char))smem;
  const int b = blockIdx.x;
  if (b < 448) {
    const int xcd = b & 7, i = b >> 3;
    const int rb = (xcd & 3) * 4 + (i & 3);
    const int cb = (i >> 2) * 2 + (xcd >> 2);
    if (cb < 10) k1_tile<0>(x, wqkv, 3 * DM, DM * 3 * DM * 2, rb, cb * 128, ws, b1, sm);
    else if (cb < 15) k1_tile<1>(x, wqkv, 3 * DM, DM * 3 * DM * 2, rb, cb * 128, ws, b1, sm);
    else if (cb < 27) k1_tile<2>(x, w1, FF, DM * FF * 2, rb, (cb - 15) * 128, ws, b1, sm);
  } else {
    const int tid = threadIdx.x, sub = tid & 15;
    float* G = (float*)(ws + WS_G);
    u32x4 gv[5];
#pragma unroll
    for (int j = 0; j < 5; ++j) gv[j] = *(const u32x4*)(gw + (j * 16 + sub) * 8);
    for (int p = 0; p < 8; ++p) {
      int row = (b - 448) * 128 + p * 16 + (tid >> 4);
      float s = 0.f;
#pragma unroll
      for (int j = 0; j < 5; ++j) {
        u32x4 v = *(const u32x4*)(x + (size_t)row * DM + (j * 16 + sub) * 8);
#pragma unroll
        for (int e = 0; e < 4; ++e) s += bflo(v[e]) * bflo(gv[j][e]) + bfhi(v[e]) * bfhi(gv[j][e]);
      }
#pragma unroll
      for (int o = 8; o > 0; o >>= 1) s += __shfl_xor(s, o);
      if (sub == 0) G[row] = 1.0f / (1.0f + __expf(-s));
    }
  }
}

template <int NSTEP, int NSLOT>
__device__ __forceinline__ void gemm160(f32x16 (&acc)[5], rsrc_t ra, u32 a0, rsrc_t rw, u32 w0, u32 ring) {
  const int lane = threadIdx.x & 63, hf = lane >> 5;
  u32 woff[5];
#pragma unroll
  for (int i = 0; i < 5; ++i) {
    int blk = 4 * i + (lane >> 4), rg = blk / 5, ct = blk - rg * 5;
    woff[i] = (4 * rg + ((lane >> 2) & 3)) * (DM * 2) + ct * 64 + (lane & 3) * 16;
  }
  const u32 aoff = lane * 16;
  auto issue = [&](int s) {
    u32 base = ring + (s % NSLOT) * 6144;
    dma16(ra, base + 5120, aoff, a0 + s * 1024);
#pragma unroll
    for (int i = 0; i < 5; ++i) dma16(rw, base + i * 1024, woff[i], w0 + s * 16 * DM * 2);
  };
  const u32 wa = ring + hf * 1280 + ((lane >> 2) & 3) * 64 + ((lane >> 4) & 1) * 32 + (lane & 3) * 8;
  const u32 aa = ring + 5120 + lane * 16;
#pragma unroll
  for (int s = 0; s < NSLOT - 1; ++s) issue(s);
#pragma unroll
  for (int s = 0; s < NSTEP; ++s) {
    if (s + NSLOT - 1 < NSTEP) issue(s + NSLOT - 1);
    const int left = NSTEP - 1 - s;
    if (left >= NSLOT - 1) wait_vm<6 * (NSLOT - 1)>();
    else if (left == 2) wait_vm<12>();
    else if (left == 1) wait_vm<6>();
    else wait_vm<0>();
    const u32 so = (s % NSLOT) * 6144;
    bf16x8 af = lds128(aa + so);
    bf16x8 wf[5];
#pragma unroll
    for (int c = 0; c < 5; ++c) wf[c] = ldstr(wa + so + c * 256, wa + so + 2560 + c * 256);
#pragma unroll
    for (int c = 0; c < 5; ++c) acc[c] = MFMA(wf[c], af, acc[c]);
    WAIT_LGKM0();
  }
}

__device__ __forceinline__ void reduce4(f32x16 (&acc)[5], f32x4 (&res)[5], u32 buf, int w, int lane) {
#pragma unroll
  for (int c = 0; c < 5; ++c)
#pragma unroll
    for (int g = 0; g < 4; ++g) {
      f32x4 v = {acc[c][4 * g], acc[c][4 * g + 1], acc[c][4 * g + 2], acc[c][4 * g + 3]};
      *(LDSP(f32x4))(buf + (((w * 5 + c) * 4 + g) * 64 + lane) * 16) = v;
    }
  __syncthreads();
#pragma unroll
  for (int c = 0; c < 5; ++c) {
    f32x4 s = {0.f, 0.f, 0.f, 0.f};
#pragma unroll
    for (int v = 0; v < 4; ++v) s += *(LDSP(f32x4))(buf + (((v * 5 + c) * 4 + w) * 64 + lane) * 16);
    res[c] = s;
  }
}

__global__ __launch_bounds__(256) void k_ffn2(const u16* w2, const u16* b2, char* ws) {
  __shared__ __attribute__((aligned(1024))) char smem[4 * 4 * 6144];
  const u32 sm = (u32)(size_t)(LDSP(char))smem;
  const int tid = threadIdx.x, lane = tid & 63, w = rfl(tid >> 6), hf = lane >> 5;
  const int b = blockIdx.x, tt = (b & 7) * 8 + ((b >> 3) >> 2), nb = (b >> 3) & 3;
  rsrc_t ra = mkrsrc(ws + WS_AH, 64 * AH_STEPS * 1024), rw = mkrsrc(w2, FF * DM * 2);
  f32x16 acc[5] = {};
  gemm160<24, 4>(acc, ra, (tt * AH_STEPS + 40 + w * 24) * 1024, rw, w * 24 * 16 * DM * 2 + nb * 320, sm + w * 4 * 6144);
  __syncthreads();
  f32x4 res[5];
  reduce4(acc, res, sm, w, lane);
  const float g1 = 1.0f - ((const float*)(ws + WS_G))[tt * 32 + (lane & 31)];
#pragma unroll
  for (int c = 0; c < 5; ++c) {
    u32x2 bb = *(const u32x2*)(b2 + nb * 160 + c * 32 + 8 * w + 4 * hf);
    f32x4 bv = {bflo(bb[0]), bfhi(bb[0]), bflo(bb[1]), bfhi(bb[1])};
    *(f32x4*)(ws + WS_BP + ((((size_t)(tt * 4 + nb) * 5 + c) * 4 + w) * 64 + lane) * 16) = (res[c] + bv) * g1;
  }
}

__global__ __launch_bounds__(256) void k3_out(const u16* wo, char* ws, u16* out) {
  __shared__ __attribute__((aligned(1024))) char smem[4 * 4 * 6144];
  const u32 sm = (u32)(size_t)(LDSP(char))smem;
  const int tid = threadIdx.x, lane = tid & 63, w = rfl(tid >> 6), hf = lane >> 5;
  const int b = blockIdx.x, tt = (b & 7) * 8 + ((b >> 3) >> 2), nb = (b >> 3) & 3;
  rsrc_t ra = mkrsrc(ws + WS_AH, 64 * AH_STEPS * 1024), rw = mkrsrc(wo, DM * DM * 2);
  f32x16 acc[5] = {};
  gemm160<10, 4>(acc, ra, (tt * AH_STEPS + w * 10) * 1024, rw, w * 10 * 16 * DM * 2 + nb * 320, sm + w * 4 * 6144);
  __syncthreads();
  f32x4 res[5];
  reduce4(acc, res, sm, w, lane);
  const int tok = tt * 32 + (lane & 31);
  const float g = ((const float*)(ws + WS_G))[tok];
#pragma unroll
  for (int c = 0; c < 5; ++c) {
    f32x4 bp = *(const f32x4*)(ws + WS_BP + ((((size_t)(tt * 4 + nb) * 5 + c) * 4 + w) * 64 + lane) * 16);
    f32x4 r = res[c] * g + bp;
    *(u32x2*)(out + (size_t)tok * DM + nb * 160 + c * 32 + 8 * w + 4 * hf) = u32x2{pk2(r[0], r[1]), pk2(r[2], r[3])};
  }
}

constexpr int AT_SLOT = 4 * KV_TILE, AT_RING = 3 * AT_SLOT;
constexpr int AT_CNT = 2 * AT_RING, AT_LDS = AT_CNT + 64;

__device__ __forceinline__ void grp_sync(u32 cnt, u32 target) {
  if ((threadIdx.x & 63) == 0) __hip_atomic_fetch_add((LDSP(u32))cnt, 1u, __ATOMIC_RELAXED, __HIP_MEMORY_SCOPE_WORKGROUP);
  while (true) {
    u32 v = rfl(__hip_atomic_load((LDSP(u32))cnt, __ATOMIC_RELAXED, __HIP_MEMORY_SCOPE_WORKGROUP));
    if (v >= target) break;
    __builtin_amdgcn_s_sleep(1);
  }
  asm volatile("" ::: "memory");
}

__device__ __forceinline__ float halfmax(float v) {
  auto r = __builtin_amdgcn_permlane32_swap(__float_as_uint(v), __float_as_uint(v), false, false);
  return fmaxf(__uint_as_float(r[0]), __uint_as_float(r[1]));
}
__device__ __forceinline__ float max16(const f32x16& s) {
  float m = fmaxf(s[0], s[1]);
#pragma unroll
  for (int i = 2; i < 16; ++i) m = fmaxf(m, s[i]);
  return m;
}
__device__ __forceinline__ void set_shift(bf16x8& qt, float m, int hf) {
  float n = -m;
  u32 a = __float_as_uint(n) & 0xffff0000u;
  float r1 = n - __uint_as_float(a);
  u32 b = __float_as_uint(r1) & 0xffff0000u;
  float r2 = r1 - __uint_as_float(b);
  u32 c = __float_as_uint(r2) & 0xffff0000u;
  u32x4 t = __builtin_bit_cast(u32x4, qt);
  t[2] = hf ? 0u : ((a >> 16) | b);
  t[3] = hf ? 0u : (c >> 16);
  qt = __builtin_bit_cast(bf16x8, t);
}

template <bool MAXPASS>
__device__ __forceinline__ void attn_pass(rsrc_t rkv, u32 src0, u32 ring, u32 cnt, u32& epoch, int wq,
                                          const bf16x8 (&qm)[2], const bf16x8 (&qt)[2], f32x16 (&o)[2], float (&mx)[2]) {
  const int lane = threadIdx.x & 63, hf = lane >> 5, l32 = lane & 31;
  const int vrow = l32 <= 20 ? l32 : (l32 < 28 ? 20 : l32 - 16);
  const u32 ka = ring + lane * 16, kt = ring + 1024 + l32 * 16, va = ring + 1536 + hf * 384 + vrow * 16;
  const u32 voff = lane * 16;
  auto issue = [&](int st) {
    u32 slot = (st % 3) * AT_SLOT + wq * 3072;
#pragma unroll
    for (int i = 0; i < 3; ++i) dma16(rkv, ring + slot + i * 1024, voff, src0 + st * AT_SLOT + wq * 3072 + i * 1024);
  };
  const f32x16 zero = {};
  issue(0);
  for (int st = 0; st < 8; ++st) {
    WAIT_VM(0);
    epoch += 4;
    grp_sync(cnt, epoch);
    if (st + 1 < 8) issue(st + 1);
    const u32 so = (st % 3) * AT_SLOT;
#pragma unroll
    for (int t = 0; t < 4; ++t) {
      bf16x8 km = lds128(ka + so + t * KV_TILE), kx = lds128(kt + so + t * KV_TILE);
      if (MAXPASS) {
#pragma unroll
        for (int j = 0; j < 2; ++j) {
          f32x16 s = MFMA(km, qm[j], zero);
          s = MFMA(kx, qt[j], s);
          mx[j] = fmaxf(mx[j], max16(s));
        }
      } else {
        bf16x8 v0 = lds128(va + so + t * KV_TILE), v1 = lds128(va + so + t * KV_TILE + 768);
#pragma unroll
        for (int j = 0; j < 2; ++j) {
          f32x16 s = MFMA(km, qm[j], zero);
          s = MFMA(kx, qt[j], s);
#pragma unroll
          for (int i = 0; i < 16; ++i) s[i] = __builtin_amdgcn_exp2f(s[i]);
          o[j] = MFMA(v0, pack8(s, 0), o[j]);
          o[j] = MFMA(v1, pack8(s, 1), o[j]);
        }
      }
    }
  }
}

__global__ __launch_bounds__(512) void k2_attn(char* ws) {
  __shared__ __attribute__((aligned(1024))) char smem[AT_LDS];
  const u32 sm = (u32)(size_t)(LDSP(char))smem;
  const int tid = threadIdx.x, lane = tid & 63, wid = rfl(tid >> 6), hf = lane >> 5, l32 = lane & 31;
  const int b = blockIdx.x, head = (b & 7) * 4 + (b >> 6), qb = (b >> 3) & 7;
  const int wq = wid & 3, kh = wid >> 2;
  if (tid < 16) *(LDSP(u32))(sm + AT_CNT + tid * 4) = 0;
  const char* kvh = ws + WS_KV + (size_t)head * 64 * KV_TILE;
  rsrc_t rkv = mkrsrc(kvh, 64 * KV_TILE);
  const int qt0 = qb * 8 + wq * 2;
  bf16x8 qm[2], qt[2];
  float mx[2];
  {
    const bf16x8 km = *(const bf16x8*)(kvh + lane * 16), kx = *(const bf16x8*)(kvh + 1024 + l32 * 16);
    const f32x16 zero = {};
#pragma unroll
    for (int j = 0; j < 2; ++j) {
      const char* q = ws + WS_Q + (size_t)(head * 64 + qt0 + j) * Q_TILE;
      qm[j] = *(const bf16x8*)(q + lane * 16);
      u32x2 t = *(const u32x2*)(q + 1024 + l32 * 16);
      qt[j] = __builtin_bit_cast(bf16x8, u32x4{hf ? 0u : t[0], hf ? 0u : t[1], 0u, 0u});
      f32x16 s = MFMA(km, qm[j], zero);
      s = MFMA(kx, qt[j], s);
      mx[j] = halfmax(max16(s));
    }
  }
  __syncthreads();
  const u32 ring = sm + kh * AT_RING, cnt = sm + AT_CNT + kh * 4;
  const u32 src0 = kh * 32 * KV_TILE;
  u32 epoch = 0;
  f32x16 o[2];
  for (int attempt = 0; attempt < 2; ++attempt) {
    set_shift(qt[0], mx[0], hf);
    set_shift(qt[1], mx[1], hf);
    o[0] = f32x16{}; o[1] = f32x16{};
    attn_pass<false>(rkv, src0, ring, cnt, epoch, wq, qm, qt, o, mx);
    __syncthreads();
    const u32 xb = sm + wq * 6144 + lane * 16;
    if (kh == 1) {
#pragma unroll
      for (int j = 0; j < 2; ++j)
#pragma unroll
        for (int g = 0; g < 3; ++g)
          *(LDSP(f32x4))(xb + (j * 3 + g) * 1024) = f32x4{o[j][4 * g], o[j][4 * g + 1], o[j][4 * g + 2], o[j][4 * g + 3]};
    }
    __syncthreads();
    int bad = 0;
    if (kh == 0) {
#pragma unroll
      for (int j = 0; j < 2; ++j) {
#pragma unroll
        for (int g = 0; g < 3; ++g) {
          f32x4 v = *(LDSP(f32x4))(xb + (j * 3 + g) * 1024);
#pragma unroll
          for (int i = 0; i < 4; ++i) o[j][4 * g + i] += v[i];
        }
        float a = 0.f;
#pragma unroll
        for (int i = 0; i < 8; ++i) a += fabsf(o[j][i]);
        if (hf == 0) {
#pragma unroll
          for (int i = 8; i < 12; ++i) a += fabsf(o[j][i]);
        } else {
          bad |= !(o[j][8] < 1.8e19f);
        }
        bad |= !(a < 3.0e38f);
      }
    }
    if (!__syncthreads_or(bad) || attempt == 1) break;
#pragma unroll
    for (int j = 0; j < 2; ++j) { set_shift(qt[j], 0.f, hf); mx[j] = -INFINITY; }
    attn_pass<true>(rkv, src0, ring, cnt, epoch, wq, qm, qt, o, mx);
    __syncthreads();
    LDSP(float) mb = (LDSP(float))(sm + (wid * 2) * 256 + lane * 4);
    mb[0] = halfmax(mx[0]); mb[64] = halfmax(mx[1]);
    __syncthreads();
    LDSP(float) mo = (LDSP(float))(sm + ((wid ^ 4) * 2) * 256 + lane * 4);
    mx[0] = fmaxf(mb[0], mo[0]); mx[1] = fmaxf(mb[64], mo[64]);
    __syncthreads();
  }
  if (kh == 0) {
#pragma unroll
    for (int j = 0; j < 2; ++j) {
      auto r = __builtin_amdgcn_permlane32_swap(__float_as_uint(o[j][8]), __float_as_uint(o[j][8]), false, false);
      const float inv = 1.0f / __uint_as_float(r[1]);
      char* dst = ws + WS_AH + (size_t)(qt0 + j) * AH_STEPS * 1024 + l32 * 16;
#pragma unroll
      for (int g = 0; g < 3; ++g) {
        if (g == 2 && hf) continue;
        const int G = head * 5 + 2 * g + hf;
        u32x2 v = {pk2(o[j][4 * g] * inv, o[j][4 * g + 1] * inv), pk2(o[j][4 * g + 2] * inv, o[j][4 * g + 3] * inv)};
        *(u32x2*)(dst + (G >> 2) * 1024 + (G & 1) * 512 + ((G >> 1) & 1) * 8) = v;
      }
    }
  }
}

extern "C" void kernel_launch(void* const* d_in, const int* in_sizes, int n_in, void* d_out, int out_size,
                              void* d_ws, size_t ws_size, hipStream_t stream) {
  const u16* x = (const u16*)d_in[0];
  const u16* wqkv = (const u16*)d_in[1];
  const u16* wo = (const u16*)d_in[2];
  const u16* w1 = (const u16*)d_in[4];
  const u16* b1 = (const u16*)d_in[5];
  const u16* w2 = (const u16*)d_in[6];
  const u16* b2 = (const u16*)d_in[7];
  const u16* gw = (const u16*)d_in[8];
  char* ws = (char*)d_ws;
  k1_proj<<<464, 256, 0, stream>>>(x, wqkv, w1, b1, gw, ws);
  k2_attn<<<256, 512, 0, stream>>>(ws);
  k_ffn2<<<256, 256, 0, stream>>>(w2, b2, ws);
  k3_out<<<256, 256, 0, stream>>>(wo, ws, (u16*)d_out);
}
```

```cpp
#include <hip/hip_runtime.h>
#include <hip/hip_bf16.h>

typedef unsigned short u16;
typedef unsigned int u32;
typedef __attribute__((ext_vector_type(8))) __bf16 bf16x8;
typedef __attribute__((ext_vector_type(2))) __bf16 bf16x2;
typedef __attribute__((ext_vector_type(4))) short s16x4;
typedef __attribute__((ext_vector_type(16))) float f32x16;
typedef __attribute__((ext_vector_type(2))) float f32x2;
typedef __attribute__((ext_vector_type(4))) float f32x4;
typedef __attribute__((ext_vector_type(4))) u32 u32x4;
typedef __attribute__((ext_vector_type(2))) u32 u32x2;
typedef __amdgpu_buffer_rsrc_t rsrc_t;
#define LDSP(T) __attribute__((address_space(3))) T*

constexpr int SEQ = 2048, DM = 640, FF = 1536, NH = 32;
constexpr u32 KV_PAIR = 5120, Q_TILE = 1280;
constexpr u32 WS_KV = 0;
constexpr u32 WS_Q = WS_KV + NH * 32 * KV_PAIR;
constexpr u32 WS_H = WS_Q + NH * 64 * Q_TILE;
constexpr u32 WS_O = WS_H + 64 * 96 * 1024;
constexpr u32 WS_W2T = WS_O + 64 * 40 * 1024;
constexpr u32 WS_WOT = WS_W2T + 4 * 96 * 5120;
constexpr u32 WS_G = WS_WOT + 4 * 40 * 5120;
constexpr u32 WS_F1 = WS_G + SEQ * 4 + 1024;
constexpr u32 WS_F2 = WS_F1 + 2048;
constexpr u32 WS_F3 = WS_F2 + 4096;
constexpr u32 WS_EP = WS_F3 + 2048;
constexpr u32 WS_END = WS_EP + 128;
constexpr float QSCALE = 0.22360679774997896f * 1.4426950408889634f;

constexpr int SC1 = 16;
#define MFMA(a, b, c) __builtin_amdgcn_mfma_f32_32x32x16_bf16((a), (b), (c), 0, 0, 0)
#define WAIT_LGKM0() asm volatile("s_waitcnt lgkmcnt(0)" ::: "memory")

__device__ __forceinline__ rsrc_t mkrsrc(const void* p, u32 bytes) {
  return __builtin_amdgcn_make_buffer_rsrc((void*)p, 0, (int)bytes, 0x00020000);
}
__device__ __forceinline__ void dma16(rsrc_t r, u32 lds, u32 voff, u32 soff) {
  asm volatile("s_mov_b32 m0, %0\n\ts_nop 0\n\tbuffer_load_dwordx4 %1, %2, %3 offen lds"
               :: "s"(lds), "v"(voff), "s"(r), "s"(soff) : "m0", "memory");
}
__device__ __forceinline__ void dma16s(rsrc_t r, u32 lds, u32 voff, u32 soff) {
  asm volatile("s_mov_b32 m0, %0\n\ts_nop 0\n\tbuffer_load_dwordx4 %1, %2, %3 offen sc1 lds"
               :: "s"(lds), "v"(voff), "s"(r), "s"(soff) : "m0", "memory");
}
template <int N> __device__ __forceinline__ void wait_vm() {
  asm volatile("s_waitcnt vmcnt(%0)" :: "n"(N) : "memory");
}
__device__ __forceinline__ u32 rfl(u32 v) { return __builtin_amdgcn_readfirstlane(v); }
__device__ __forceinline__ u32 pk2(float a, float b) {
  f32x2 v = {a, b};
  return __builtin_bit_cast(u32, __builtin_convertvector(v, bf16x2));
}
__device__ __forceinline__ float bflo(u32 v) { return __uint_as_float(v << 16); }
__device__ __forceinline__ float bfhi(u32 v) { return __uint_as_float(v & 0xffff0000u); }
__device__ __forceinline__ bf16x8 lds128(u32 a) { return *(LDSP(bf16x8))a; }
__device__ __forceinline__ bf16x8 ldstr(u32 a0, u32 a1) {
  s16x4 lo = __builtin_amdgcn_ds_read_tr16_b64_v4i16((LDSP(s16x4))a0);
  s16x4 hi = __builtin_amdgcn_ds_read_tr16_b64_v4i16((LDSP(s16x4))a1);
  return __builtin_bit_cast(bf16x8, __builtin_shufflevector(lo, hi, 0, 1, 2, 3, 4, 5, 6, 7));
}
__device__ __forceinline__ bf16x8 pack8(const f32x16& a, int s) {
  u32x4 r = {pk2(a[8 * s], a[8 * s + 1]), pk2(a[8 * s + 2], a[8 * s + 3]),
             pk2(a[8 * s + 4], a[8 * s + 5]), pk2(a[8 * s + 6], a[8 * s + 7])};
  return __builtin_bit_cast(bf16x8, r);
}

constexpr int F1AT = 12, W2RD = 14, W2ST = 18;
constexpr u32 CODEB = 44000;
#define MFMA16(a, b, c) __builtin_amdgcn_mfma_f32_16x16x32_bf16((a), (b), (c), 0, 0, 0)
constexpr u32 K1_RING = 81920, K1_SLOT = 24576, K1_AUX = K1_RING + 3 * K1_SLOT;
__device__ const __attribute__((aligned(16))) u32 QKTAB[10][4] = {{0x0b0a0100u, 0x08070605u, 0x17160d0cu, 0x09040302u}, {0x100f1211u, 0x1f1e1514u, 0x1c1b1a19u, 0x1d18130eu}, {0x04030605u, 0x0b0a0908u, 0x100f0e0du, 0x07020100u}, {0x13121514u, 0x18171a19u, 0x1f1e1d1cu, 0x1b16110cu}, {0x04030201u, 0x07060908u, 0x0c0b0e0du, 0x0f0a0500u}, {0x13121110u, 0x18171615u, 0x1b1a1d1cu, 0x1f1e1914u}, {0x03020100u, 0x07060504u, 0x0f0e1110u, 0x17080a09u}, {0x0c0b1615u, 0x1b1a1918u, 0x14131e1du, 0x1f1c120du}, {0x03020504u, 0x08070a09u, 0x0f0e0d0cu, 0x0b060100u}, {0x14131211u, 0x17161918u, 0x1c1b1e1du, 0x1f1a1510u}};

__device__ __forceinline__ void st16(rsrc_t r, u32x4 v, u32 voff, u32 soff) { __builtin_amdgcn_raw_buffer_store_b128(v, r, voff, soff, SC1); }
__device__ __forceinline__ void st8(rsrc_t r, u32x2 v, u32 voff, u32 soff) { __builtin_amdgcn_raw_buffer_store_b64(v, r, voff, soff, SC1); }
__device__ __forceinline__ void bar() { asm volatile("s_barrier" ::: "memory"); }
__device__ __forceinline__ u32x2 calltag(u32x2 e) {
  asm volatile("" : "+v"(e));
  return __builtin_bit_cast(u32x2, __builtin_bit_cast(unsigned long long, e) + 0x9e3779b97f4a7c15ull);
}
__device__ __forceinline__ u32x4 pairimg(const f32x16& a, int p) {
  auto lo = __builtin_amdgcn_permlane16_swap(pk2(a[8 * p], a[8 * p + 1]), pk2(a[8 * p + 4], a[8 * p + 5]), false, false);
  auto hi = __builtin_amdgcn_permlane16_swap(pk2(a[8 * p + 2], a[8 * p + 3]), pk2(a[8 * p + 6], a[8 * p + 7]), false, false);
  return u32x4{lo[0], hi[0], lo[1], hi[1]};
}
__device__ __forceinline__ void wunit_issue(rsrc_t rw, u32 row0, u32 row1, u32 cu, u32 lds) {
  const int lane = threadIdx.x & 63;
  const u32 c = cu * 128 + (((lane & 7) ^ ((lane >> 2) & 4)) << 4);
  dma16(rw, lds, row0 * (DM * 2) + c, 0);
  dma16(rw, lds + 1024, row1 * (DM * 2) + c, 0);
}
__device__ __forceinline__ u32x4 wunit_read(int f, u32 lds) {
  const int lane = threadIdx.x & 63, r = (lane >> 2) & 3;
  const u32 a = lds + (lane >> 5) * 1024 + r * 128 + (((4 * f + ((lane >> 3) & 2) + ((lane >> 1) & 1)) ^ ((r & 2) << 1)) << 4) + (lane & 1) * 8;
  return __builtin_bit_cast(u32x4, ldstr(a, a + 512));
}
__device__ __forceinline__ void wunit_store(rsrc_t rs, u32 dst, u32 nst, u32 st, u32 cu, int f, u32x4 v) {
  const u32 c32 = 2 * cu + f, nb = c32 / 5;
  st16(rs, v, (threadIdx.x & 63) * 16, dst + (nb * nst + st) * 5120 + (c32 - nb * 5) * 1024);
}

constexpr u32 AT_PAD = 128, AT_PITCH = KV_PAIR + AT_PAD, AT_RING = 12 * AT_PITCH + AT_PAD, AT_PART = 24576, AT_X = AT_RING + 4 * AT_PART;
constexpr u32 AT_Q = AT_PAD + 8 * AT_PITCH;
constexpr u32 AT_LOW = 12288, AT_BLK = 4224;
constexpr int SPIN = 1 << 18;
typedef __attribute__((ext_vector_type(16))) __bf16 bf16x16;
typedef __attribute__((ext_vector_type(8))) float f32x8;

__device__ __forceinline__ float halfmax(float v) {
  auto r = __builtin_amdgcn_permlane32_swap(__float_as_uint(v), __float_as_uint(v), false, false);
  return fmaxf(__uint_as_float(r[0]), __uint_as_float(r[1]));
}
__device__ __forceinline__ float max16(const f32x16& s) {
  float m = fmaxf(s[0], s[1]);
#pragma unroll
  for (int i = 2; i < 16; ++i) m = fmaxf(m, s[i]);
  return m;
}
__device__ __forceinline__ f32x16 qk(bf16x8 km, s16x4 kx, bf16x8 qm, s16x4 qt, f32x16 c) {
  return __builtin_amdgcn_mfma_f32_32x32x8bf16_1k(kx, qt, MFMA(km, qm, c), 0, 0, 0);
}
__device__ __forceinline__ void finish(u32 sm, u32 c, u32 ln, rsrc_t ro) {
  const u32 tg = ln >> 3, g = (ln >> 1) & 3, h = ln & 1, col = c * 64 + (ln & 7) * 8;
  const u32 base = sm + (c < 2 ? AT_RING + c * AT_BLK : AT_PAD + (c - 2) * 4 * AT_PITCH) + g * 1024 + ((g + 1) >> 1) * 64 + h * 128 + tg * 16;
  const u32 vs = c < 2 ? AT_LOW : AT_PITCH;
  const f32x4 gt = *(LDSP(f32x4))(sm + AT_X + 1024 + tg * 16);
  const u32x2 bb = *(LDSP(u32x2))(sm + AT_X + col);
  f32x4 p[4][4];
#pragma unroll
  for (int j = 0; j < 4; ++j)
#pragma unroll
    for (int v = 0; v < 4; ++v) p[j][v] = *(LDSP(f32x4))(base + v * vs + j * 256);
  asm volatile("" ::: "memory");
  const f32x4 g1 = 1.0f - gt;
  f32x4 s[4];
#pragma unroll
  for (int j = 0; j < 4; ++j) s[j] = g1 * (j & 1 ? bfhi(bb[j >> 1]) : bflo(bb[j >> 1])) + p[j][0] + p[j][1] + (p[j][2] + p[j][3]);
#pragma unroll
  for (int i = 0; i < 4; ++i)
    __builtin_amdgcn_raw_buffer_store_b64(u32x2{pk2(s[0][i], s[1][i]), pk2(s[2][i], s[3][i])}, ro, (4 * tg + i) * (DM * 2) + col, 0, SC1);
}

__global__ __launch_bounds__(768, 3) void fused(const u16* x, const u16* wqkv, const u16* w1, const u16* b1, const u16* gw,
                                                const u16* w2, const u16* wo, const u16* b2, char* ws, u16* out) {
  __shared__ __attribute__((aligned(1024))) char smem[163840];
  const u32 sm = (u32)(size_t)(LDSP(char))smem;
  const int tid = threadIdx.x, lane = tid & 63;
  int wid = rfl(tid >> 6);
  const int b = blockIdx.x, rb = ((b >> 2) & 1) * 8 + (b >> 5), cb = (b & 3) * 4 + ((b >> 3) & 3);
  const int half = cb & 1, fb = cb >> 1;
  const bool spare = cb == 15;
  const int head = (b & 7) * 4 + (b >> 6), qb = (b >> 3) & 7;
  const char* kvh = ws + WS_KV + (size_t)head * 32 * KV_PAIR;
  const rsrc_t rs = mkrsrc(ws, WS_END);
  const u32 rot = 5 * (4 * (cb & 3) + (cb >> 2) + 2 * (rb & 7) + (rb >> 3)) / 8 % 10;
  auto kst = [&](int s) { const u32 k = s % 10 + rot; return k < 10 ? k : k - 10; };
  const int l = wid & 3, rl = lane >> 3, cq8 = lane & 7;
  const rsrc_t rx = mkrsrc(x, SEQ * DM * 2), rw = mkrsrc(wqkv, DM * 3 * DM * 2), rf = mkrsrc(w1, DM * FF * 2);
  const u32 xo0 = (16 * l + rl) * (DM * 2) + ((cq8 ^ (rl >> 1)) << 4), xo1 = (16 * l + 8 + rl) * (DM * 2) + ((cq8 ^ (4 + (rl >> 1))) << 4);
  const u32 ch = (cq8 ^ ((rl & 2) + 4 * (l & 1))) << 4;
  const u32 wo_ = (8 * l + rl) * (3 * DM * 2) + ch, fo = (8 * l + rl) * (FF * 2) + ch;
  const u32 xr = (rb * 128 + half * 64) * (DM * 2), xs = (rb * 128 + (half ^ 1) * 64) * (DM * 2);
  auto resident = [&](int s) {
    dma16(rx, sm + s * 8192 + l * 2048, xo0, xr + kst(s) * 128);
    dma16(rx, sm + s * 8192 + l * 2048 + 1024, xo1, xr + kst(s) * 128);
  };
  auto stage = [&](int s) {
    const u32 slot = sm + K1_RING + (s % 3) * K1_SLOT, k = kst(s);
    if (s < 10) {
      if (!spare) {
#pragma unroll
        for (int i = 0; i < 4; ++i)
          dma16(rw, slot + l * 4096 + i * 1024, wo_, (64 * k + 32 * (i >> 1)) * (3 * DM * 2) + cb * 256 + (i & 1) * 128);
      }
      dma16(rx, slot + 16384 + l * 2048, xo0, xs + k * 128);
      dma16(rx, slot + 16384 + l * 2048 + 1024, xo1, xs + k * 128);
    } else {
#pragma unroll
      for (int i = 0; i < 6; ++i)
        dma16(rf, slot + l * 6144 + i * 1024, fo, (64 * k + 32 * (i / 3)) * (FF * 2) + fb * 384 + (i % 3) * 128);
    }
  };
  if (__builtin_expect(wid >= 8, 1)) {
#pragma unroll
    for (int s = 0; s < 3; ++s) { stage(s); resident(s); }
  }
  u32 touch = 0;
  if (wid == 4 && lane < 16) {
    unsigned long long pc;
    asm volatile("s_getpc_b64 %0" : "=s"(pc));
    const u32 o = ((b >> 3) * 16 + lane) * 128;
    touch = *(const __attribute__((address_space(1))) u32*)(pc + (o < CODEB ? o : CODEB));
  }
  asm volatile("" : "+s"(wid));
  if (wid >= 8) {
    u32x2 prev;
    asm volatile("buffer_load_dwordx2 %0, off, %1, %2 sc1" : "=v"(prev) : "s"(rs), "s"(WS_EP) : "memory");
    bar();
    if (spare) wait_vm<9>(); else wait_vm<17>();
    bar();
    u32 l16 = lane * 16;
    auto step = [&](int s) {
      if (s == 10) {}
      else if (s >= 9) wait_vm<6>();
      else if (s == 1) { if (spare) wait_vm<5>(); else wait_vm<9>(); }
      else if (spare) wait_vm<4>();
      else wait_vm<8>();
      bar();
      stage(s + 2);
      if (s < 8) resident(s + 2);
    };
#pragma unroll
    for (int s = 1; s < 11; ++s) step(s);
    const int w = wid - 8;
    const int tt = (b & 7) * 8 + (b >> 5), nb = (b >> 3) & 3;
    u32 sh = WS_H + (tt * 96 + w * 24) * 1024, sw = WS_W2T + (nb * 96 + w * 24) * 5120, t0, t1;
    rsrc_t ro = mkrsrc(out + (size_t)tt * 32 * DM + nb * 160, 31 * DM * 2 + 320);
    rsrc_t rkv = mkrsrc(kvh, 32 * KV_PAIR), rb2 = mkrsrc(b2 + nb * 160, 320);
    u32 qd[3], qs[3];
#pragma unroll
    for (int i = 0; i < 3; ++i) {
      const u32 u = 3 * w + i;
      qd[i] = sm + AT_Q + u * 1024 + (u * 13 >> 6) * AT_PAD;
      qs[i] = u < 10 ? WS_Q + (head * 64 + qb * 8) * Q_TILE + u * 1024 : WS_G + (qb + u - 10) * 1024;
      asm volatile("" : "+s"(qd[i]), "+s"(qs[i]));
    }
    u32 kvd = sm + AT_PAD + w * AT_PITCH, kvs = w * KV_PAIR, sg = WS_G + tt * 128, sd = sm + AT_RING + w * AT_LOW;
    u32 sp = WS_WOT + (nb * 40 + 10 * w) * 5120, so = WS_O + (tt * 40 + 10 * w) * 1024;
    u32 sf = WS_F2 + (2 * rb + half) * 128 + fb * 8, pf = WS_F2 + (tt >> 1) * 128, s3 = WS_F3 + b * 8;
    asm volatile("" : "+s"(sh), "+s"(sw), "+s"(ro), "+s"(rkv), "+s"(rb2), "+s"(kvd), "+s"(kvs), "+s"(sg), "+s"(sd), "+s"(sp), "+s"(so),
                      "+s"(sf), "+s"(pf), "+s"(s3));
    const u32x2 tag = calltag(prev);
    u32 tl = rfl(tag[0]), th = rfl(tag[1]);
    asm volatile("" : "+s"(tl), "+s"(th));
    wait_vm<12>(); bar();
#pragma unroll
    for (int s = 11; s < 18; ++s) step(s);
    auto fetch = [&]() {
#pragma unroll
      for (int i = 0; i < 3; ++i) dma16s(rs, qd[i], l16, qs[i]);
#pragma unroll
      for (int i = 0; i < 10; ++i) dma16s(rkv, kvd + (i / 5) * 4 * AT_PITCH + (i % 5) * 1024, l16, kvs + (i / 5) * 4 * KV_PAIR + (i % 5) * 1024);
    };
    {
      u32x2 fq;
      auto pollq = [&]() {
        asm volatile("buffer_load_dwordx2 %0, %1, %2, %3 offen sc1" : "=&v"(fq) : "v"(l16 >> 1), "s"(rs), "s"(WS_F1 + w * 512) : "memory");
      };
      auto missq = [&]() {
        asm volatile("" : "+v"(fq));
        return __builtin_amdgcn_ballot_w64(((fq[0] ^ tl) | (fq[1] ^ th)) != 0) != 0;
      };
      pollq();
      wait_vm<7>(); bar();
      wait_vm<0>();
#pragma unroll 1
      for (int n = 0; n < SPIN && missq(); ++n) {
        __builtin_amdgcn_s_sleep(2);
        pollq();
        wait_vm<0>();
      }
      bar();
      fetch();
    }
    f32x16 acc[5];
    asm volatile(
      "v_mov_b32 v2, %[tl]\n"
      "v_mov_b32 v3, %[th]\n"
      "v_and_b32 v1, 48, v0\n"
      "s_waitcnt vmcnt(5)\n"
      "s_barrier\n"
      "s_cmp_lg_u32 %[kvs], 0\n"
      "s_cbranch_scc1 5f\n"
      "s_mov_b64 exec, 1\n"
      "buffer_store_dwordx2 v[2:3], off, %[rs], %[s3] sc1\n"
      "s_mov_b64 exec, -1\n"
      "5:\n"
      "s_waitcnt vmcnt(0)\n"
      "s_barrier\n"
      "s_cmp_lg_u32 %[kvs], 0\n"
      "s_cbranch_scc1 5f\n"
      "s_mov_b64 exec, 1\n"
      "buffer_store_dwordx2 v[2:3], off, %[rs], %[sf] sc1\n"
      "s_cmp_lg_u32 %[sf], %[f2]\n"
      "s_cbranch_scc1 8f\n"
      "s_mov_b32 %[t0], %[ep]\n"
      "buffer_store_dwordx2 v[2:3], off, %[rs], %[t0] sc1\n"
      "8:\n"
      "s_mov_b64 exec, -1\n"
      "5:\n"
      "s_add_u32 m0, %[kvd], 41984\n"
      "s_add_u32 %[t0], %[kvs], 40960\n"
      "buffer_load_dwordx4 v0, %[rkv], %[t0] offen offset:0 sc1 lds\n"
      "buffer_load_dwordx4 v0, %[rkv], %[t0] offen offset:1024 sc1 lds\n"
      "buffer_load_dwordx4 v0, %[rkv], %[t0] offen offset:2048 sc1 lds\n"
      "buffer_load_dwordx4 v0, %[rkv], %[t0] offen offset:3072 sc1 lds\n"
      "s_add_u32 m0, %[kvd], 46080\n"
      "s_add_u32 %[t0], %[kvs], 45056\n"
      "buffer_load_dwordx4 v0, %[rkv], %[t0] offen offset:0 sc1 lds\n"
      "s_sleep 12\n"
      "s_mov_b32 %[t0], %[f3]\n"
      "buffer_load_dwordx4 v[144:147], v0, %[rs], %[t0] offen offset:0 sc1\n"
      "buffer_load_dwordx4 v[148:151], v0, %[rs], %[t0] offen offset:1024 sc1\n"
      "buffer_load_dwordx4 v[152:155], v1, %[rs], %[pf] offen sc1\n"
      "s_setprio 3\n"
      "s_waitcnt vmcnt(1)\n"
      "s_mov_b32 %[t1], 0x40000\n"
      "6:\n"
      "v_xor_b32 v144, %[tl], v144\n"
      "v_xor_b32 v145, %[th], v145\n"
      "v_xor_b32 v146, %[tl], v146\n"
      "v_xor_b32 v147, %[th], v147\n"
      "v_xor_b32 v148, %[tl], v148\n"
      "v_xor_b32 v149, %[th], v149\n"
      "v_xor_b32 v150, %[tl], v150\n"
      "v_xor_b32 v151, %[th], v151\n"
      "v_or3_b32 v144, v144, v145, v146\n"
      "v_or3_b32 v147, v147, v148, v149\n"
      "v_or3_b32 v144, v144, v150, v151\n"
      "v_or_b32 v144, v144, v147\n"
      "v_cmp_ne_u32 vcc, 0, v144\n"
      "s_cbranch_vccz 7f\n"
      "s_sub_u32 %[t1], %[t1], 1\n"
      "s_cbranch_scc1 7f\n"
      "s_sleep 8\n"
      "s_mov_b32 %[t0], %[f3]\n"
      "buffer_load_dwordx4 v[144:147], v0, %[rs], %[t0] offen offset:0 sc1\n"
      "buffer_load_dwordx4 v[148:151], v0, %[rs], %[t0] offen offset:1024 sc1\n"
      "s_waitcnt vmcnt(0)\n"
      "s_branch 6b\n"
      "7:\n"
      "s_waitcnt vmcnt(0)\n"
      "s_mov_b32 %[t1], 0x40000\n"
      "6:\n"
      "v_xor_b32 v152, %[tl], v152\n"
      "v_xor_b32 v153, %[th], v153\n"
      "v_xor_b32 v154, %[tl], v154\n"
      "v_xor_b32 v155, %[th], v155\n"
      "v_or3_b32 v152, v152, v153, v154\n"
      "v_or_b32 v152, v152, v155\n"
      "v_cmp_ne_u32 vcc, 0, v152\n"
      "s_cbranch_vccz 7f\n"
      "s_sub_u32 %[t1], %[t1], 1\n"
      "s_cbranch_scc1 7f\n"
      "s_sleep 2\n"
      "buffer_load_dwordx4 v[152:155], v1, %[rs], %[pf] offen sc1\n"
      "s_waitcnt vmcnt(0)\n"
      "s_branch 6b\n"
      "7:\n"
      "v_bfe_u32 v2, v0, 4, 5\n"
      "v_lshlrev_b32 v2, 2, v2\n"
      "s_setprio 0\n"
      "buffer_load_dwordx4 v[84:87], v0, %[rs], %[sw] offen offset:0 sc1\n"
      "buffer_load_dwordx4 v[88:91], v0, %[rs], %[sw] offen offset:1024 sc1\n"
      "buffer_load_dwordx4 v[92:95], v0, %[rs], %[sw] offen offset:2048 sc1\n"
      "buffer_load_dwordx4 v[96:99], v0, %[rs], %[sw] offen offset:3072 sc1\n"
      "s_add_u32 %[t0], %[sw], 4096\n"
      "buffer_load_dwordx4 v[100:103], v0, %[rs], %[t0] offen sc1\n"
      "buffer_load_dwordx4 v[104:107], v0, %[rs], %[sh] offen sc1\n"
      "s_add_u32 %[sw], %[sw], 5120\n"
      "s_add_u32 %[sh], %[sh], 1024\n"
      "buffer_load_dwordx4 v[108:111], v0, %[rs], %[sw] offen offset:0 sc1\n"
      "buffer_load_dwordx4 v[112:115], v0, %[rs], %[sw] offen offset:1024 sc1\n"
      "buffer_load_dwordx4 v[116:119], v0, %[rs], %[sw] offen offset:2048 sc1\n"
      "buffer_load_dwordx4 v[120:123], v0, %[rs], %[sw] offen offset:3072 sc1\n"
      "s_add_u32 %[t0], %[sw], 4096\n"
      "buffer_load_dwordx4 v[124:127], v0, %[rs], %[t0] offen sc1\n"
      "buffer_load_dwordx4 v[128:131], v0, %[rs], %[sh] offen sc1\n"
      "s_add_u32 %[sw], %[sw], 5120\n"
      "s_add_u32 %[sh], %[sh], 1024\n"
      "s_barrier\n"
      "s_add_u32 m0, %[kvd], 0\n"
      "s_add_u32 %[t0], %[kvs], 61440\n"
      "buffer_load_dwordx4 v0, %[rkv], %[t0] offen offset:0 sc1 lds\n"
      "buffer_load_dwordx4 v0, %[rkv], %[t0] offen offset:1024 sc1 lds\n"
      "buffer_load_dwordx4 v0, %[rkv], %[t0] offen offset:2048 sc1 lds\n"
      "buffer_load_dwordx4 v0, %[rkv], %[t0] offen offset:3072 sc1 lds\n"
      "s_add_u32 m0, %[kvd], 4096\n"
      "s_add_u32 %[t0], %[kvs], 65536\n"
      "buffer_load_dwordx4 v0, %[rkv], %[t0] offen offset:0 sc1 lds\n"
      "buffer_load_dwordx4 v[132:135], v0, %[rs], %[sw] offen offset:0 sc1\n"
      "buffer_load_dwordx4 v[136:139], v0, %[rs], %[sw] offen offset:1024 sc1\n"
      "buffer_load_dwordx4 v[140:143], v0, %[rs], %[sw] offen offset:2048 sc1\n"
      "buffer_load_dwordx4 v[144:147], v0, %[rs], %[sw] offen offset:3072 sc1\n"
      "s_add_u32 %[t0], %[sw], 4096\n"
      "buffer_load_dwordx4 v[148:151], v0, %[rs], %[t0] offen sc1\n"
      "buffer_load_dwordx4 v[152:155], v0, %[rs], %[sh] offen sc1\n"
      "s_add_u32 %[sw], %[sw], 5120\n"
      "s_add_u32 %[sh], %[sh], 1024\n"
      "buffer_load_dword v1, v2, %[rs], %[sg] offen sc1\n"
      "s_waitcnt vmcnt(18)\n"
      "v_mfma_f32_32x32x16_bf16 v[4:19], v[84:87], v[104:107], 0\n"
      "buffer_load_dwordx4 v[84:87], v0, %[rs], %[sw] offen offset:0 sc1\n"
      "s_sleep 1\n"
      "v_mfma_f32_32x32x16_bf16 v[20:35], v[88:91], v[104:107], 0\n"
      "buffer_load_dwordx4 v[88:91], v0, %[rs], %[sw] offen offset:1024 sc1\n"
      "s_sleep 1\n"
      "v_mfma_f32_32x32x16_bf16 v[36:51], v[92:95], v[104:107], 0\n"
      "buffer_load_dwordx4 v[92:95], v0, %[rs], %[sw] offen offset:2048 sc1\n"
      "s_sleep 1\n"
      "v_mfma_f32_32x32x16_bf16 v[52:67], v[96:99], v[104:107], 0\n"
      "buffer_load_dwordx4 v[96:99], v0, %[rs], %[sw] offen offset:3072 sc1\n"
      "s_sleep 1\n"
      "v_mfma_f32_32x32x16_bf16 v[68:83], v[100:103], v[104:107], 0\n"
      "s_add_u32 %[t0], %[sw], 4096\n"
      "buffer_load_dwordx4 v[100:103], v0, %[rs], %[t0] offen sc1\n"
      "s_sleep 1\n"
      "buffer_load_dwordx4 v[104:107], v0, %[rs], %[sh] offen sc1\n"
      "s_add_u32 %[sw], %[sw], 5120\n"
      "s_add_u32 %[sh], %[sh], 1024\n"
      "s_waitcnt vmcnt(18)\n"
      "v_mfma_f32_32x32x16_bf16 v[4:19], v[108:111], v[128:131], v[4:19]\n"
      "buffer_load_dwordx4 v[108:111], v0, %[rs], %[sw] offen offset:0 sc1\n"
      "s_sleep 1\n"
      "v_mfma_f32_32x32x16_bf16 v[20:35], v[112:115], v[128:131], v[20:35]\n"
      "buffer_load_dwordx4 v[112:115], v0, %[rs], %[sw] offen offset:1024 sc1\n"
      "s_sleep 1\n"
      "v_mfma_f32_32x32x16_bf16 v[36:51], v[116:119], v[128:131], v[36:51]\n"
      "buffer_load_dwordx4 v[116:119], v0, %[rs], %[sw] offen offset:2048 sc1\n"
      "s_sleep 1\n"
      "v_mfma_f32_32x32x16_bf16 v[52:67], v[120:123], v[128:131], v[52:67]\n"
      "buffer_load_dwordx4 v[120:123], v0, %[rs], %[sw] offen offset:3072 sc1\n"
      "s_sleep 1\n"
      "v_mfma_f32_32x32x16_bf16 v[68:83], v[124:127], v[128:131], v[68:83]\n"
      "s_add_u32 %[t0], %[sw], 4096\n"
      "buffer_load_dwordx4 v[124:127], v0, %[rs], %[t0] offen sc1\n"
      "s_sleep 1\n"
      "buffer_load_dwordx4 v[128:131], v0, %[rs], %[sh] offen sc1\n"
      "s_add_u32 %[sw], %[sw], 5120\n"
      "s_add_u32 %[sh], %[sh], 1024\n"
      "s_waitcnt vmcnt(13)\n"
      "v_mfma_f32_32x32x16_bf16 v[4:19], v[132:135], v[152:155], v[4:19]\n"
      "buffer_load_dwordx4 v[132:135], v0, %[rs], %[sw] offen offset:0 sc1\n"
      "s_sleep 1\n"
      "v_mfma_f32_32x32x16_bf16 v[20:35], v[136:139], v[152:155], v[20:35]\n"
      "buffer_load_dwordx4 v[136:139], v0, %[rs], %[sw] offen offset:1024 sc1\n"
      "s_sleep 1\n"
      "v_mfma_f32_32x32x16_bf16 v[36:51], v[140:143], v[152:155], v[36:51]\n"
      "buffer_load_dwordx4 v[140:143], v0, %[rs], %[sw] offen offset:2048 sc1\n"
      "s_sleep 1\n"
      "v_mfma_f32_32x32x16_bf16 v[52:67], v[144:147], v[152:155], v[52:67]\n"
      "buffer_load_dwordx4 v[144:147], v0, %[rs], %[sw] offen offset:3072 sc1\n"
      "s_sleep 1\n"
      "v_mfma_f32_32x32x16_bf16 v[68:83], v[148:151], v[152:155], v[68:83]\n"
      "s_add_u32 %[t0], %[sw], 4096\n"
      "buffer_load_dwordx4 v[148:151], v0, %[rs], %[t0] offen sc1\n"
      "s_sleep 1\n"
      "buffer_load_dwordx4 v[152:155], v0, %[rs], %[sh] offen sc1\n"
      "s_add_u32 %[sw], %[sw], 5120\n"
      "s_add_u32 %[sh], %[sh], 1024\n"
      "s_waitcnt vmcnt(12)\n"
      "v_mfma_f32_32x32x16_bf16 v[4:19], v[84:87], v[104:107], v[4:19]\n"
      "buffer_load_dwordx4 v[84:87], v0, %[rs], %[sw] offen offset:0 sc1\n"
      "s_sleep 1\n"
      "v_mfma_f32_32x32x16_bf16 v[20:35], v[88:91], v[104:107], v[20:35]\n"
      "buffer_load_dwordx4 v[88:91], v0, %[rs], %[sw] offen offset:1024 sc1\n"
      "s_sleep 1\n"
      "v_mfma_f32_32x32x16_bf16 v[36:51], v[92:95], v[104:107], v[36:51]\n"
      "buffer_load_dwordx4 v[92:95], v0, %[rs], %[sw] offen offset:2048 sc1\n"
      "s_sleep 1\n"
      "v_mfma_f32_32x32x16_bf16 v[52:67], v[96:99], v[104:107], v[52:67]\n"
      "buffer_load_dwordx4 v[96:99], v0, %[rs], %[sw] offen offset:3072 sc1\n"
      "s_sleep 1\n"
      "v_mfma_f32_32x32x16_bf16 v[68:83], v[100:103], v[104:107], v[68:83]\n"
      "s_add_u32 %[t0], %[sw], 4096\n"
      "buffer_load_dwordx4 v[100:103], v0, %[rs], %[t0] offen sc1\n"
      "s_sleep 1\n"
      "buffer_load_dwordx4 v[104:107], v0, %[rs], %[sh] offen sc1\n"
      "s_add_u32 %[sw], %[sw], 5120\n"
      "s_add_u32 %[sh], %[sh], 1024\n"
      "s_barrier\n"
      "s_add_u32 m0, %[kvd], 20992\n"
      "s_add_u32 %[t0], %[kvs], 81920\n"
      "buffer_load_dwordx4 v0, %[rkv], %[t0] offen offset:0 sc1 lds\n"
      "buffer_load_dwordx4 v0, %[rkv], %[t0] offen offset:1024 sc1 lds\n"
      "buffer_load_dwordx4 v0, %[rkv], %[t0] offen offset:2048 sc1 lds\n"
      "buffer_load_dwordx4 v0, %[rkv], %[t0] offen offset:3072 sc1 lds\n"
      "s_add_u32 m0, %[kvd], 25088\n"
      "s_add_u32 %[t0], %[kvs], 86016\n"
      "buffer_load_dwordx4 v0, %[rkv], %[t0] offen offset:0 sc1 lds\n"
      "s_waitcnt vmcnt(17)\n"
      "v_mfma_f32_32x32x16_bf16 v[4:19], v[108:111], v[128:131], v[4:19]\n"
      "buffer_load_dwordx4 v[108:111], v0, %[rs], %[sw] offen offset:0 sc1\n"
      "s_sleep 1\n"
      "v_mfma_f32_32x32x16_bf16 v[20:35], v[112:115], v[128:131], v[20:35]\n"
      "buffer_load_dwordx4 v[112:115], v0, %[rs], %[sw] offen offset:1024 sc1\n"
      "s_sleep 1\n"
      "v_mfma_f32_32x32x16_bf16 v[36:51], v[116:119], v[128:131], v[36:51]\n"
      "buffer_load_dwordx4 v[116:119], v0, %[rs], %[sw] offen offset:2048 sc1\n"
      "s_sleep 1\n"
      "v_mfma_f32_32x32x16_bf16 v[52:67], v[120:123], v[128:131], v[52:67]\n"
      "buffer_load_dwordx4 v[120:123], v0, %[rs], %[sw] offen offset:3072 sc1\n"
      "s_sleep 1\n"
      "v_mfma_f32_32x32x16_bf16 v[68:83], v[124:127], v[128:131], v[68:83]\n"
      "s_add_u32 %[t0], %[sw], 4096\n"
      "buffer_load_dwordx4 v[124:127], v0, %[rs], %[t0] offen sc1\n"
      "s_sleep 1\n"
      "buffer_load_dwordx4 v[128:131], v0, %[rs], %[sh] offen sc1\n"
      "s_add_u32 %[sw], %[sw], 5120\n"
      "s_add_u32 %[sh], %[sh], 1024\n"
      "s_waitcnt vmcnt(17)\n"
      "v_mfma_f32_32x32x16_bf16 v[4:19], v[132:135], v[152:155], v[4:19]\n"
      "buffer_load_dwordx4 v[132:135], v0, %[rs], %[sw] offen offset:0 sc1\n"
      "s_sleep 1\n"
      "v_mfma_f32_32x32x16_bf16 v[20:35], v[136:139], v[152:155], v[20:35]\n"
      "buffer_load_dwordx4 v[136:139], v0, %[rs], %[sw] offen offset:1024 sc1\n"
      "s_sleep 1\n"
      "v_mfma_f32_32x32x16_bf16 v[36:51], v[140:143], v[152:155], v[36:51]\n"
      "buffer_load_dwordx4 v[140:143], v0, %[rs], %[sw] offen offset:2048 sc1\n"
      "s_sleep 1\n"
      "v_mfma_f32_32x32x16_bf16 v[52:67], v[144:147], v[152:155], v[52:67]\n"
      "buffer_load_dwordx4 v[144:147], v0, %[rs], %[sw] offen offset:3072 sc1\n"
      "s_sleep 1\n"
      "v_mfma_f32_32x32x16_bf16 v[68:83], v[148:151], v[152:155], v[68:83]\n"
      "s_add_u32 %[t0], %[sw], 4096\n"
      "buffer_load_dwordx4 v[148:151], v0, %[rs], %[t0] offen sc1\n"
      "s_sleep 1\n"
      "buffer_load_dwordx4 v[152:155], v0, %[rs], %[sh] offen sc1\n"
      "s_add_u32 %[sw], %[sw], 5120\n"
      "s_add_u32 %[sh], %[sh], 1024\n"
      "s_waitcnt vmcnt(17)\n"
      "v_mfma_f32_32x32x16_bf16 v[4:19], v[84:87], v[104:107], v[4:19]\n"
      "buffer_load_dwordx4 v[84:87], v0, %[rs], %[sw] offen offset:0 sc1\n"
      "s_sleep 1\n"
      "v_mfma_f32_32x32x16_bf16 v[20:35], v[88:91], v[104:107], v[20:35]\n"
      "buffer_load_dwordx4 v[88:91], v0, %[rs], %[sw] offen offset:1024 sc1\n"
      "s_sleep 1\n"
      "v_mfma_f32_32x32x16_bf16 v[36:51], v[92:95], v[104:107], v[36:51]\n"
      "buffer_load_dwordx4 v[92:95], v0, %[rs], %[sw] offen offset:2048 sc1\n"
      "s_sleep 1\n"
      "v_mfma_f32_32x32x16_bf16 v[52:67], v[96:99], v[104:107], v[52:67]\n"
      "buffer_load_dwordx4 v[96:99], v0, %[rs], %[sw] offen offset:3072 sc1\n"
      "s_sleep 1\n"
      "v_mfma_f32_32x32x16_bf16 v[68:83], v[100:103], v[104:107], v[68:83]\n"
      "s_add_u32 %[t0], %[sw], 4096\n"
      "buffer_load_dwordx4 v[100:103], v0, %[rs], %[t0] offen sc1\n"
      "s_sleep 1\n"
      "buffer_load_dwordx4 v[104:107], v0, %[rs], %[sh] offen sc1\n"
      "s_add_u32 %[sw], %[sw], 5120\n"
      "s_add_u32 %[sh], %[sh], 1024\n"
      "s_waitcnt vmcnt(12)\n"
      "v_mfma_f32_32x32x16_bf16 v[4:19], v[108:111], v[128:131], v[4:19]\n"
      "buffer_load_dwordx4 v[108:111], v0, %[rs], %[sw] offen offset:0 sc1\n"
      "s_sleep 1\n"
      "v_mfma_f32_32x32x16_bf16 v[20:35], v[112:115], v[128:131], v[20:35]\n"
      "buffer_load_dwordx4 v[112:115], v0, %[rs], %[sw] offen offset:1024 sc1\n"
      "s_sleep 1\n"
      "v_mfma_f32_32x32x16_bf16 v[36:51], v[116:119], v[128:131], v[36:51]\n"
      "buffer_load_dwordx4 v[116:119], v0, %[rs], %[sw] offen offset:2048 sc1\n"
      "s_sleep 1\n"
      "v_mfma_f32_32x32x16_bf16 v[52:67], v[120:123], v[128:131], v[52:67]\n"
      "buffer_load_dwordx4 v[120:123], v0, %[rs], %[sw] offen offset:3072 sc1\n"
      "s_sleep 1\n"
      "v_mfma_f32_32x32x16_bf16 v[68:83], v[124:127], v[128:131], v[68:83]\n"
      "s_add_u32 %[t0], %[sw], 4096\n"
      "buffer_load_dwordx4 v[124:127], v0, %[rs], %[t0] offen sc1\n"
      "s_sleep 1\n"
      "buffer_load_dwordx4 v[128:131], v0, %[rs], %[sh] offen sc1\n"
      "s_add_u32 %[sw], %[sw], 5120\n"
      "s_add_u32 %[sh], %[sh], 1024\n"
      "s_add_u32 m0, %[sd], 0\n"
      "s_add_u32 %[t0], %[sp], 11264\n"
      "buffer_load_dwordx4 v0, %[rs], %[t0] offen offset:0 sc1 lds\n"
      "buffer_load_dwordx4 v0, %[rs], %[t0] offen offset:1024 sc1 lds\n"
      "buffer_load_dwordx4 v0, %[rs], %[t0] offen offset:2048 sc1 lds\n"
      "buffer_load_dwordx4 v0, %[rs], %[t0] offen offset:3072 sc1 lds\n"
      "s_add_u32 m0, %[sd], 4096\n"
      "s_add_u32 %[t0], %[sp], 15360\n"
      "buffer_load_dwordx4 v0, %[rs], %[t0] offen offset:0 sc1 lds\n"
      "buffer_load_dwordx4 v0, %[rs], %[t0] offen offset:1024 sc1 lds\n"
      "s_barrier\n"
      "s_add_u32 m0, %[kvd], 41984\n"
      "s_add_u32 %[t0], %[kvs], 102400\n"
      "buffer_load_dwordx4 v0, %[rkv], %[t0] offen offset:0 sc1 lds\n"
      "buffer_load_dwordx4 v0, %[rkv], %[t0] offen offset:1024 sc1 lds\n"
      "buffer_load_dwordx4 v0, %[rkv], %[t0] offen offset:2048 sc1 lds\n"
      "buffer_load_dwordx4 v0, %[rkv], %[t0] offen offset:3072 sc1 lds\n"
      "s_add_u32 m0, %[kvd], 46080\n"
      "s_add_u32 %[t0], %[kvs], 106496\n"
      "buffer_load_dwordx4 v0, %[rkv], %[t0] offen offset:0 sc1 lds\n"
      "s_waitcnt vmcnt(23)\n"
      "v_mfma_f32_32x32x16_bf16 v[4:19], v[132:135], v[152:155], v[4:19]\n"
      "buffer_load_dwordx4 v[132:135], v0, %[rs], %[sw] offen offset:0 sc1\n"
      "s_sleep 1\n"
      "v_mfma_f32_32x32x16_bf16 v[20:35], v[136:139], v[152:155], v[20:35]\n"
      "buffer_load_dwordx4 v[136:139], v0, %[rs], %[sw] offen offset:1024 sc1\n"
      "s_sleep 1\n"
      "v_mfma_f32_32x32x16_bf16 v[36:51], v[140:143], v[152:155], v[36:51]\n"
      "buffer_load_dwordx4 v[140:143], v0, %[rs], %[sw] offen offset:2048 sc1\n"
      "s_sleep 1\n"
      "v_mfma_f32_32x32x16_bf16 v[52:67], v[144:147], v[152:155], v[52:67]\n"
      "buffer_load_dwordx4 v[144:147], v0, %[rs], %[sw] offen offset:3072 sc1\n"
      "s_sleep 1\n"
      "v_mfma_f32_32x32x16_bf16 v[68:83], v[148:151], v[152:155], v[68:83]\n"
      "s_add_u32 %[t0], %[sw], 4096\n"
      "buffer_load_dwordx4 v[148:151], v0, %[rs], %[t0] offen sc1\n"
      "s_sleep 1\n"
      "buffer_load_dwordx4 v[152:155], v0, %[rs], %[sh] offen sc1\n"
      "s_add_u32 %[sw], %[sw], 5120\n"
      "s_add_u32 %[sh], %[sh], 1024\n"
      "s_waitcnt vmcnt(23)\n"
      "v_mfma_f32_32x32x16_bf16 v[4:19], v[84:87], v[104:107], v[4:19]\n"
      "buffer_load_dwordx4 v[84:87], v0, %[rs], %[sw] offen offset:0 sc1\n"
      "s_sleep 1\n"
      "v_mfma_f32_32x32x16_bf16 v[20:35], v[88:91], v[104:107], v[20:35]\n"
      "buffer_load_dwordx4 v[88:91], v0, %[rs], %[sw] offen offset:1024 sc1\n"
      "s_sleep 1\n"
      "v_mfma_f32_32x32x16_bf16 v[36:51], v[92:95], v[104:107], v[36:51]\n"
      "buffer_load_dwordx4 v[92:95], v0, %[rs], %[sw] offen offset:2048 sc1\n"
      "s_sleep 1\n"
      "v_mfma_f32_32x32x16_bf16 v[52:67], v[96:99], v[104:107], v[52:67]\n"
      "buffer_load_dwordx4 v[96:99], v0, %[rs], %[sw] offen offset:3072 sc1\n"
      "s_sleep 1\n"
      "v_mfma_f32_32x32x16_bf16 v[68:83], v[100:103], v[104:107], v[68:83]\n"
      "s_add_u32 %[t0], %[sw], 4096\n"
      "buffer_load_dwordx4 v[100:103], v0, %[rs], %[t0] offen sc1\n"
      "s_sleep 1\n"
      "buffer_load_dwordx4 v[104:107], v0, %[rs], %[sh] offen sc1\n"
      "s_add_u32 %[sw], %[sw], 5120\n"
      "s_add_u32 %[sh], %[sh], 1024\n"
      "s_waitcnt vmcnt(23)\n"
      "v_mfma_f32_32x32x16_bf16 v[4:19], v[108:111], v[128:131], v[4:19]\n"
      "buffer_load_dwordx4 v[108:111], v0, %[rs], %[sw] offen offset:0 sc1\n"
      "s_sleep 1\n"
      "v_mfma_f32_32x32x16_bf16 v[20:35], v[112:115], v[128:131], v[20:35]\n"
      "buffer_load_dwordx4 v[112:115], v0, %[rs], %[sw] offen offset:1024 sc1\n"
      "s_sleep 1\n"
      "v_mfma_f32_32x32x16_bf16 v[36:51], v[116:119], v[128:131], v[36:51]\n"
      "buffer_load_dwordx4 v[116:119], v0, %[rs], %[sw] offen offset:2048 sc1\n"
      "s_sleep 1\n"
      "v_mfma_f32_32x32x16_bf16 v[52:67], v[120:123], v[128:131], v[52:67]\n"
      "buffer_load_dwordx4 v[120:123], v0, %[rs], %[sw] offen offset:3072 sc1\n"
      "s_sleep 1\n"
      "v_mfma_f32_32x32x16_bf16 v[68:83], v[124:127], v[128:131], v[68:83]\n"
      "s_add_u32 %[t0], %[sw], 4096\n"
      "buffer_load_dwordx4 v[124:127], v0, %[rs], %[t0] offen sc1\n"
      "s_sleep 1\n"
      "buffer_load_dwordx4 v[128:131], v0, %[rs], %[sh] offen sc1\n"
      "s_add_u32 %[sw], %[sw], 5120\n"
      "s_add_u32 %[sh], %[sh], 1024\n"
      "s_waitcnt vmcnt(12)\n"
      "v_mfma_f32_32x32x16_bf16 v[4:19], v[132:135], v[152:155], v[4:19]\n"
      "buffer_load_dwordx4 v[132:135], v0, %[rs], %[sw] offen offset:0 sc1\n"
      "s_sleep 1\n"
      "v_mfma_f32_32x32x16_bf16 v[20:35], v[136:139], v[152:155], v[20:35]\n"
      "buffer_load_dwordx4 v[136:139], v0, %[rs], %[sw] offen offset:1024 sc1\n"
      "s_sleep 1\n"
      "v_mfma_f32_32x32x16_bf16 v[36:51], v[140:143], v[152:155], v[36:51]\n"
      "buffer_load_dwordx4 v[140:143], v0, %[rs], %[sw] offen offset:2048 sc1\n"
      "s_sleep 1\n"
      "v_mfma_f32_32x32x16_bf16 v[52:67], v[144:147], v[152:155], v[52:67]\n"
      "buffer_load_dwordx4 v[144:147], v0, %[rs], %[sw] offen offset:3072 sc1\n"
      "s_sleep 1\n"
      "v_mfma_f32_32x32x16_bf16 v[68:83], v[148:151], v[152:155], v[68:83]\n"
      "s_add_u32 %[t0], %[sw], 4096\n"
      "buffer_load_dwordx4 v[148:151], v0, %[rs], %[t0] offen sc1\n"
      "s_sleep 1\n"
      "buffer_load_dwordx4 v[152:155], v0, %[rs], %[sh] offen sc1\n"
      "s_add_u32 %[sw], %[sw], 5120\n"
      "s_add_u32 %[sh], %[sh], 1024\n"
      "s_add_u32 m0, %[sd], 6144\n"
      "s_add_u32 %[t0], %[sp], 17408\n"
      "buffer_load_dwordx4 v0, %[rs], %[t0] offen offset:0 sc1 lds\n"
      "buffer_load_dwordx4 v0, %[rs], %[t0] offen offset:1024 sc1 lds\n"
      "buffer_load_dwordx4 v0, %[rs], %[t0] offen offset:2048 sc1 lds\n"
      "buffer_load_dwordx4 v0, %[rs], %[t0] offen offset:3072 sc1 lds\n"
      "s_add_u32 m0, %[sd], 10240\n"
      "s_add_u32 %[t0], %[sp], 21504\n"
      "buffer_load_dwordx4 v0, %[rs], %[t0] offen offset:0 sc1 lds\n"
      "buffer_load_dwordx4 v0, %[rs], %[t0] offen offset:1024 sc1 lds\n"
      "s_barrier\n"
      "s_add_u32 m0, %[kvd], 0\n"
      "s_add_u32 %[t0], %[kvs], 122880\n"
      "buffer_load_dwordx4 v0, %[rkv], %[t0] offen offset:0 sc1 lds\n"
      "buffer_load_dwordx4 v0, %[rkv], %[t0] offen offset:1024 sc1 lds\n"
      "buffer_load_dwordx4 v0, %[rkv], %[t0] offen offset:2048 sc1 lds\n"
      "buffer_load_dwordx4 v0, %[rkv], %[t0] offen offset:3072 sc1 lds\n"
      "s_add_u32 m0, %[kvd], 4096\n"
      "s_add_u32 %[t0], %[kvs], 126976\n"
      "buffer_load_dwordx4 v0, %[rkv], %[t0] offen offset:0 sc1 lds\n"
      "s_waitcnt vmcnt(23)\n"
      "v_mfma_f32_32x32x16_bf16 v[4:19], v[84:87], v[104:107], v[4:19]\n"
      "buffer_load_dwordx4 v[84:87], v0, %[rs], %[sw] offen offset:0 sc1\n"
      "v_mfma_f32_32x32x16_bf16 v[20:35], v[88:91], v[104:107], v[20:35]\n"
      "buffer_load_dwordx4 v[88:91], v0, %[rs], %[sw] offen offset:1024 sc1\n"
      "s_sleep 1\n"
      "v_mfma_f32_32x32x16_bf16 v[36:51], v[92:95], v[104:107], v[36:51]\n"
      "buffer_load_dwordx4 v[92:95], v0, %[rs], %[sw] offen offset:2048 sc1\n"
      "v_mfma_f32_32x32x16_bf16 v[52:67], v[96:99], v[104:107], v[52:67]\n"
      "buffer_load_dwordx4 v[96:99], v0, %[rs], %[sw] offen offset:3072 sc1\n"
      "s_sleep 1\n"
      "v_mfma_f32_32x32x16_bf16 v[68:83], v[100:103], v[104:107], v[68:83]\n"
      "s_add_u32 %[t0], %[sw], 4096\n"
      "buffer_load_dwordx4 v[100:103], v0, %[rs], %[t0] offen sc1\n"
      "s_sleep 1\n"
      "buffer_load_dwordx4 v[104:107], v0, %[rs], %[sh] offen sc1\n"
      "s_add_u32 %[sw], %[sw], 5120\n"
      "s_add_u32 %[sh], %[sh], 1024\n"
      "s_waitcnt vmcnt(23)\n"
      "v_mfma_f32_32x32x16_bf16 v[4:19], v[108:111], v[128:131], v[4:19]\n"
      "buffer_load_dwordx4 v[108:111], v0, %[rs], %[sw] offen offset:0 sc1\n"
      "v_mfma_f32_32x32x16_bf16 v[20:35], v[112:115], v[128:131], v[20:35]\n"
      "buffer_load_dwordx4 v[112:115], v0, %[rs], %[sw] offen offset:1024 sc1\n"
      "s_sleep 1\n"
      "v_mfma_f32_32x32x16_bf16 v[36:51], v[116:119], v[128:131], v[36:51]\n"
      "buffer_load_dwordx4 v[116:119], v0, %[rs], %[sw] offen offset:2048 sc1\n"
      "v_mfma_f32_32x32x16_bf16 v[52:67], v[120:123], v[128:131], v[52:67]\n"
      "buffer_load_dwordx4 v[120:123], v0, %[rs], %[sw] offen offset:3072 sc1\n"
      "s_sleep 1\n"
      "v_mfma_f32_32x32x16_bf16 v[68:83], v[124:127], v[128:131], v[68:83]\n"
      "s_add_u32 %[t0], %[sw], 4096\n"
      "buffer_load_dwordx4 v[124:127], v0, %[rs], %[t0] offen sc1\n"
      "s_sleep 1\n"
      "buffer_load_dwordx4 v[128:131], v0, %[rs], %[sh] offen sc1\n"
      "s_add_u32 %[sw], %[sw], 5120\n"
      "s_add_u32 %[sh], %[sh], 1024\n"
      "s_waitcnt vmcnt(23)\n"
      "v_mfma_f32_32x32x16_bf16 v[4:19], v[132:135], v[152:155], v[4:19]\n"
      "buffer_load_dwordx4 v[132:135], v0, %[rs], %[sw] offen offset:0 sc1\n"
      "v_mfma_f32_32x32x16_bf16 v[20:35], v[136:139], v[152:155], v[20:35]\n"
      "buffer_load_dwordx4 v[136:139], v0, %[rs], %[sw] offen offset:1024 sc1\n"
      "s_sleep 1\n"
      "v_mfma_f32_32x32x16_bf16 v[36:51], v[140:143], v[152:155], v[36:51]\n"
      "buffer_load_dwordx4 v[140:143], v0, %[rs], %[sw] offen offset:2048 sc1\n"
      "v_mfma_f32_32x32x16_bf16 v[52:67], v[144:147], v[152:155], v[52:67]\n"
      "buffer_load_dwordx4 v[144:147], v0, %[rs], %[sw] offen offset:3072 sc1\n"
      "s_sleep 1\n"
      "v_mfma_f32_32x32x16_bf16 v[68:83], v[148:151], v[152:155], v[68:83]\n"
      "s_add_u32 %[t0], %[sw], 4096\n"
      "buffer_load_dwordx4 v[148:151], v0, %[rs], %[t0] offen sc1\n"
      "s_sleep 1\n"
      "buffer_load_dwordx4 v[152:155], v0, %[rs], %[sh] offen sc1\n"
      "s_add_u32 %[sw], %[sw], 5120\n"
      "s_add_u32 %[sh], %[sh], 1024\n"
      "s_waitcnt vmcnt(12)\n"
      "v_mfma_f32_32x32x16_bf16 v[4:19], v[84:87], v[104:107], v[4:19]\n"
      "buffer_load_dwordx4 v[84:87], v0, %[rs], %[sw] offen offset:0 sc1\n"
      "v_mfma_f32_32x32x16_bf16 v[20:35], v[88:91], v[104:107], v[20:35]\n"
      "buffer_load_dwordx4 v[88:91], v0, %[rs], %[sw] offen offset:1024 sc1\n"
      "s_sleep 1\n"
      "v_mfma_f32_32x32x16_bf16 v[36:51], v[92:95], v[104:107], v[36:51]\n"
      "buffer_load_dwordx4 v[92:95], v0, %[rs], %[sw] offen offset:2048 sc1\n"
      "v_mfma_f32_32x32x16_bf16 v[52:67], v[96:99], v[104:107], v[52:67]\n"
      "buffer_load_dwordx4 v[96:99], v0, %[rs], %[sw] offen offset:3072 sc1\n"
      "s_sleep 1\n"
      "v_mfma_f32_32x32x16_bf16 v[68:83], v[100:103], v[104:107], v[68:83]\n"
      "s_add_u32 %[t0], %[sw], 4096\n"
      "buffer_load_dwordx4 v[100:103], v0, %[rs], %[t0] offen sc1\n"
      "s_sleep 1\n"
      "buffer_load_dwordx4 v[104:107], v0, %[rs], %[sh] offen sc1\n"
      "s_add_u32 %[sw], %[sw], 5120\n"
      "s_add_u32 %[sh], %[sh], 1024\n"
      "s_waitcnt vmcnt(12)\n"
      "v_mfma_f32_32x32x16_bf16 v[4:19], v[108:111], v[128:131], v[4:19]\n"
      "buffer_load_dwordx4 v[108:111], v0, %[rs], %[sw] offen offset:0 sc1\n"
      "v_mfma_f32_32x32x16_bf16 v[20:35], v[112:115], v[128:131], v[20:35]\n"
      "buffer_load_dwordx4 v[112:115], v0, %[rs], %[sw] offen offset:1024 sc1\n"
      "s_sleep 1\n"
      "v_mfma_f32_32x32x16_bf16 v[36:51], v[116:119], v[128:131], v[36:51]\n"
      "buffer_load_dwordx4 v[116:119], v0, %[rs], %[sw] offen offset:2048 sc1\n"
      "v_mfma_f32_32x32x16_bf16 v[52:67], v[120:123], v[128:131], v[52:67]\n"
      "buffer_load_dwordx4 v[120:123], v0, %[rs], %[sw] offen offset:3072 sc1\n"
      "s_sleep 1\n"
      "v_mfma_f32_32x32x16_bf16 v[68:83], v[124:127], v[128:131], v[68:83]\n"
      "s_add_u32 %[t0], %[sw], 4096\n"
      "buffer_load_dwordx4 v[124:127], v0, %[rs], %[t0] offen sc1\n"
      "s_sleep 1\n"
      "buffer_load_dwordx4 v[128:131], v0, %[rs], %[sh] offen sc1\n"
      "s_add_u32 %[sw], %[sw], 5120\n"
      "s_add_u32 %[sh], %[sh], 1024\n"
      "s_add_u32 m0, %[sd], 49152\n"
      "s_add_u32 %[t0], %[sp], 23552\n"
      "buffer_load_dwordx4 v0, %[rs], %[t0] offen offset:0 sc1 lds\n"
      "buffer_load_dwordx4 v0, %[rs], %[t0] offen offset:1024 sc1 lds\n"
      "buffer_load_dwordx4 v0, %[rs], %[t0] offen offset:2048 sc1 lds\n"
      "buffer_load_dwordx4 v0, %[rs], %[t0] offen offset:3072 sc1 lds\n"
      "s_add_u32 m0, %[sd], 53248\n"
      "s_add_u32 %[t0], %[sp], 27648\n"
      "buffer_load_dwordx4 v0, %[rs], %[t0] offen offset:0 sc1 lds\n"
      "buffer_load_dwordx4 v0, %[rs], %[t0] offen offset:1024 sc1 lds\n"
      "s_barrier\n"
      "s_add_u32 m0, %[kvd], 20992\n"
      "s_add_u32 %[t0], %[kvs], 143360\n"
      "buffer_load_dwordx4 v0, %[rkv], %[t0] offen offset:0 sc1 lds\n"
      "buffer_load_dwordx4 v0, %[rkv], %[t0] offen offset:1024 sc1 lds\n"
      "buffer_load_dwordx4 v0, %[rkv], %[t0] offen offset:2048 sc1 lds\n"
      "buffer_load_dwordx4 v0, %[rkv], %[t0] offen offset:3072 sc1 lds\n"
      "s_add_u32 m0, %[kvd], 25088\n"
      "s_add_u32 %[t0], %[kvs], 147456\n"
      "buffer_load_dwordx4 v0, %[rkv], %[t0] offen offset:0 sc1 lds\n"
      "s_waitcnt vmcnt(23)\n"
      "v_mfma_f32_32x32x16_bf16 v[4:19], v[132:135], v[152:155], v[4:19]\n"
      "buffer_load_dwordx4 v[132:135], v0, %[rs], %[sw] offen offset:0 sc1\n"
      "v_mfma_f32_32x32x16_bf16 v[20:35], v[136:139], v[152:155], v[20:35]\n"
      "buffer_load_dwordx4 v[136:139], v0, %[rs], %[sw] offen offset:1024 sc1\n"
      "s_sleep 1\n"
      "v_mfma_f32_32x32x16_bf16 v[36:51], v[140:143], v[152:155], v[36:51]\n"
      "buffer_load_dwordx4 v[140:143], v0, %[rs], %[sw] offen offset:2048 sc1\n"
      "v_mfma_f32_32x32x16_bf16 v[52:67], v[144:147], v[152:155], v[52:67]\n"
      "buffer_load_dwordx4 v[144:147], v0, %[rs], %[sw] offen offset:3072 sc1\n"
      "s_sleep 1\n"
      "v_mfma_f32_32x32x16_bf16 v[68:83], v[148:151], v[152:155], v[68:83]\n"
      "s_add_u32 %[t0], %[sw], 4096\n"
      "buffer_load_dwordx4 v[148:151], v0, %[rs], %[t0] offen sc1\n"
      "s_sleep 1\n"
      "buffer_load_dwordx4 v[152:155], v0, %[rs], %[sh] offen sc1\n"
      "s_add_u32 %[sw], %[sw], 5120\n"
      "s_add_u32 %[sh], %[sh], 1024\n"
      "s_waitcnt vmcnt(23)\n"
      "v_mfma_f32_32x32x16_bf16 v[4:19], v[84:87], v[104:107], v[4:19]\n"
      "buffer_load_dwordx4 v[84:87], v0, %[rs], %[sw] offen offset:0 sc1\n"
      "v_mfma_f32_32x32x16_bf16 v[20:35], v[88:91], v[104:107], v[20:35]\n"
      "buffer_load_dwordx4 v[88:91], v0, %[rs], %[sw] offen offset:1024 sc1\n"
      "s_sleep 1\n"
      "v_mfma_f32_32x32x16_bf16 v[36:51], v[92:95], v[104:107], v[36:51]\n"
      "buffer_load_dwordx4 v[92:95], v0, %[rs], %[sw] offen offset:2048 sc1\n"
      "v_mfma_f32_32x32x16_bf16 v[52:67], v[96:99], v[104:107], v[52:67]\n"
      "buffer_load_dwordx4 v[96:99], v0, %[rs], %[sw] offen offset:3072 sc1\n"
      "s_sleep 1\n"
      "v_mfma_f32_32x32x16_bf16 v[68:83], v[100:103], v[104:107], v[68:83]\n"
      "s_add_u32 %[t0], %[sw], 4096\n"
      "buffer_load_dwordx4 v[100:103], v0, %[rs], %[t0] offen sc1\n"
      "s_sleep 1\n"
      "buffer_load_dwordx4 v[104:107], v0, %[rs], %[sh] offen sc1\n"
      "s_add_u32 %[sw], %[sw], 5120\n"
      "s_add_u32 %[sh], %[sh], 1024\n"
      "s_waitcnt vmcnt(23)\n"
      "v_mfma_f32_32x32x16_bf16 v[4:19], v[108:111], v[128:131], v[4:19]\n"
      "buffer_load_dwordx4 v[108:111], v0, %[rs], %[sw] offen offset:0 sc1\n"
      "v_mfma_f32_32x32x16_bf16 v[20:35], v[112:115], v[128:131], v[20:35]\n"
      "buffer_load_dwordx4 v[112:115], v0, %[rs], %[sw] offen offset:1024 sc1\n"
      "s_sleep 1\n"
      "v_mfma_f32_32x32x16_bf16 v[36:51], v[116:119], v[128:131], v[36:51]\n"
      "buffer_load_dwordx4 v[116:119], v0, %[rs], %[sw] offen offset:2048 sc1\n"
      "v_mfma_f32_32x32x16_bf16 v[52:67], v[120:123], v[128:131], v[52:67]\n"
      "buffer_load_dwordx4 v[120:123], v0, %[rs], %[sw] offen offset:3072 sc1\n"
      "s_sleep 1\n"
      "v_mfma_f32_32x32x16_bf16 v[68:83], v[124:127], v[128:131], v[68:83]\n"
      "s_add_u32 %[t0], %[sw], 4096\n"
      "buffer_load_dwordx4 v[124:127], v0, %[rs], %[t0] offen sc1\n"
      "s_sleep 1\n"
      "buffer_load_dwordx4 v[128:131], v0, %[rs], %[sh] offen sc1\n"
      "s_add_u32 %[sw], %[sw], 5120\n"
      "s_add_u32 %[sh], %[sh], 1024\n"
      "s_waitcnt vmcnt(12)\n"
      "v_mfma_f32_32x32x16_bf16 v[4:19], v[132:135], v[152:155], v[4:19]\n"
      "buffer_load_dwordx4 v[132:135], v0, %[rs], %[sw] offen offset:0 sc1\n"
      "v_mfma_f32_32x32x16_bf16 v[20:35], v[136:139], v[152:155], v[20:35]\n"
      "buffer_load_dwordx4 v[136:139], v0, %[rs], %[sw] offen offset:1024 sc1\n"
      "s_sleep 1\n"
      "v_mfma_f32_32x32x16_bf16 v[36:51], v[140:143], v[152:155], v[36:51]\n"
      "buffer_load_dwordx4 v[140:143], v0, %[rs], %[sw] offen offset:2048 sc1\n"
      "v_mfma_f32_32x32x16_bf16 v[52:67], v[144:147], v[152:155], v[52:67]\n"
      "buffer_load_dwordx4 v[144:147], v0, %[rs], %[sw] offen offset:3072 sc1\n"
      "s_sleep 1\n"
      "v_mfma_f32_32x32x16_bf16 v[68:83], v[148:151], v[152:155], v[68:83]\n"
      "s_add_u32 %[t0], %[sw], 4096\n"
      "buffer_load_dwordx4 v[148:151], v0, %[rs], %[t0] offen sc1\n"
      "s_sleep 1\n"
      "buffer_load_dwordx4 v[152:155], v0, %[rs], %[sh] offen sc1\n"
      "s_add_u32 %[sw], %[sw], 5120\n"
      "s_add_u32 %[sh], %[sh], 1024\n"
      "s_waitcnt vmcnt(12)\n"
      "v_mfma_f32_32x32x16_bf16 v[4:19], v[84:87], v[104:107], v[4:19]\n"
      "v_mfma_f32_32x32x16_bf16 v[20:35], v[88:91], v[104:107], v[20:35]\n"
      "s_sleep 1\n"
      "v_mfma_f32_32x32x16_bf16 v[36:51], v[92:95], v[104:107], v[36:51]\n"
      "v_mfma_f32_32x32x16_bf16 v[52:67], v[96:99], v[104:107], v[52:67]\n"
      "s_sleep 1\n"
      "v_mfma_f32_32x32x16_bf16 v[68:83], v[100:103], v[104:107], v[68:83]\n"
      "s_sleep 1\n"
      "s_add_u32 m0, %[sd], 55296\n"
      "s_add_u32 %[t0], %[sp], 29696\n"
      "buffer_load_dwordx4 v0, %[rs], %[t0] offen offset:0 sc1 lds\n"
      "buffer_load_dwordx4 v0, %[rs], %[t0] offen offset:1024 sc1 lds\n"
      "buffer_load_dwordx4 v0, %[rs], %[t0] offen offset:2048 sc1 lds\n"
      "buffer_load_dwordx4 v0, %[rs], %[t0] offen offset:3072 sc1 lds\n"
      "s_add_u32 m0, %[sd], 59392\n"
      "s_add_u32 %[t0], %[sp], 33792\n"
      "buffer_load_dwordx4 v0, %[rs], %[t0] offen offset:0 sc1 lds\n"
      "buffer_load_dwordx4 v0, %[rs], %[t0] offen offset:1024 sc1 lds\n"
      "s_mov_b32 m0, %[sx]\n"
      "s_nop 0\n"
      "buffer_load_dwordx4 v0, %[rb], 0 offen lds\n"
      "s_add_u32 m0, %[sx], 1024\n"
      "s_nop 0\n"
      "buffer_load_dwordx4 v0, %[rs], %[sg] offen sc1 lds\n"
      "s_barrier\n"
      "s_waitcnt vmcnt(14)\n"
      "v_mfma_f32_32x32x16_bf16 v[4:19], v[108:111], v[128:131], v[4:19]\n"
      "s_sleep 1\n"
      "v_mfma_f32_32x32x16_bf16 v[20:35], v[112:115], v[128:131], v[20:35]\n"
      "v_mfma_f32_32x32x16_bf16 v[36:51], v[116:119], v[128:131], v[36:51]\n"
      "s_sleep 1\n"
      "v_mfma_f32_32x32x16_bf16 v[52:67], v[120:123], v[128:131], v[52:67]\n"
      "v_mfma_f32_32x32x16_bf16 v[68:83], v[124:127], v[128:131], v[68:83]\n"
      "s_sleep 1\n"
      "s_waitcnt vmcnt(8)\n"
      "v_mfma_f32_32x32x16_bf16 v[4:19], v[132:135], v[152:155], v[4:19]\n"
      "s_sleep 1\n"
      "v_mfma_f32_32x32x16_bf16 v[20:35], v[136:139], v[152:155], v[20:35]\n"
      "v_mfma_f32_32x32x16_bf16 v[36:51], v[140:143], v[152:155], v[36:51]\n"
      "s_sleep 1\n"
      "v_mfma_f32_32x32x16_bf16 v[52:67], v[144:147], v[152:155], v[52:67]\n"
      "v_mfma_f32_32x32x16_bf16 v[68:83], v[148:151], v[152:155], v[68:83]\n"
      "s_sleep 1\n"
      "s_add_u32 m0, %[kvd], 41984\n"
      "s_add_u32 %[t0], %[sp], 35840\n"
      "buffer_load_dwordx4 v0, %[rs], %[t0] offen offset:0 sc1 lds\n"
      "buffer_load_dwordx4 v0, %[rs], %[t0] offen offset:1024 sc1 lds\n"
      "buffer_load_dwordx4 v0, %[rs], %[t0] offen offset:2048 sc1 lds\n"
      "buffer_load_dwordx4 v0, %[rs], %[t0] offen offset:3072 sc1 lds\n"
      "s_add_u32 m0, %[kvd], 46080\n"
      "s_add_u32 %[t0], %[sp], 39936\n"
      "buffer_load_dwordx4 v0, %[rs], %[t0] offen offset:0 sc1 lds\n"
      "s_add_u32 %[t0], %[sp], 0\n"
      "buffer_load_dwordx4 v[124:127], v0, %[rs], %[t0] offen offset:0 sc1\n"
      "buffer_load_dwordx4 v[128:131], v0, %[rs], %[t0] offen offset:1024 sc1\n"
      "buffer_load_dwordx4 v[132:135], v0, %[rs], %[t0] offen offset:2048 sc1\n"
      "buffer_load_dwordx4 v[136:139], v0, %[rs], %[t0] offen offset:3072 sc1\n"
      "s_add_u32 %[t0], %[sp], 4096\n"
      "buffer_load_dwordx4 v[140:143], v0, %[rs], %[t0] offen offset:0 sc1\n"
      "buffer_load_dwordx4 v[144:147], v0, %[rs], %[t0] offen offset:1024 sc1\n"
      "buffer_load_dwordx4 v[148:151], v0, %[rs], %[t0] offen offset:2048 sc1\n"
      "buffer_load_dwordx4 v[152:155], v0, %[rs], %[t0] offen offset:3072 sc1\n"
      "s_add_u32 %[t0], %[sp], 8192\n"
      "buffer_load_dwordx4 v[156:159], v0, %[rs], %[t0] offen offset:0 sc1\n"
      "buffer_load_dwordx4 v[160:163], v0, %[rs], %[t0] offen offset:1024 sc1\n"
      "buffer_load_dwordx4 v[164:167], v0, %[rs], %[t0] offen offset:2048 sc1\n"
      "s_setprio 0\n"
      "v_sub_f32 v1, 1.0, v1\n"
      "s_nop 7\n"
      "s_nop 7\n"
      "v_mul_f32 v4, v1, v4\n"
      "v_mul_f32 v5, v1, v5\n"
      "v_mul_f32 v6, v1, v6\n"
      "v_mul_f32 v7, v1, v7\n"
      "v_mul_f32 v8, v1, v8\n"
      "v_mul_f32 v9, v1, v9\n"
      "v_mul_f32 v10, v1, v10\n"
      "v_mul_f32 v11, v1, v11\n"
      "v_mul_f32 v12, v1, v12\n"
      "v_mul_f32 v13, v1, v13\n"
      "v_mul_f32 v14, v1, v14\n"
      "v_mul_f32 v15, v1, v15\n"
      "v_mul_f32 v16, v1, v16\n"
      "v_mul_f32 v17, v1, v17\n"
      "v_mul_f32 v18, v1, v18\n"
      "v_mul_f32 v19, v1, v19\n"
      "v_mul_f32 v20, v1, v20\n"
      "v_mul_f32 v21, v1, v21\n"
      "v_mul_f32 v22, v1, v22\n"
      "v_mul_f32 v23, v1, v23\n"
      "v_mul_f32 v24, v1, v24\n"
      "v_mul_f32 v25, v1, v25\n"
      "v_mul_f32 v26, v1, v26\n"
      "v_mul_f32 v27, v1, v27\n"
      "v_mul_f32 v28, v1, v28\n"
      "v_mul_f32 v29, v1, v29\n"
      "v_mul_f32 v30, v1, v30\n"
      "v_mul_f32 v31, v1, v31\n"
      "v_mul_f32 v32, v1, v32\n"
      "v_mul_f32 v33, v1, v33\n"
      "v_mul_f32 v34, v1, v34\n"
      "v_mul_f32 v35, v1, v35\n"
      "v_mul_f32 v36, v1, v36\n"
      "v_mul_f32 v37, v1, v37\n"
      "v_mul_f32 v38, v1, v38\n"
      "v_mul_f32 v39, v1, v39\n"
      "v_mul_f32 v40, v1, v40\n"
      "v_mul_f32 v41, v1, v41\n"
      "v_mul_f32 v42, v1, v42\n"
      "v_mul_f32 v43, v1, v43\n"
      "v_mul_f32 v44, v1, v44\n"
      "v_mul_f32 v45, v1, v45\n"
      "v_mul_f32 v46, v1, v46\n"
      "v_mul_f32 v47, v1, v47\n"
      "v_mul_f32 v48, v1, v48\n"
      "v_mul_f32 v49, v1, v49\n"
      "v_mul_f32 v50, v1, v50\n"
      "v_mul_f32 v51, v1, v51\n"
      "v_mul_f32 v52, v1, v52\n"
      "v_mul_f32 v53, v1, v53\n"
      "v_mul_f32 v54, v1, v54\n"
      "v_mul_f32 v55, v1, v55\n"
      "v_mul_f32 v56, v1, v56\n"
      "v_mul_f32 v57, v1, v57\n"
      "v_mul_f32 v58, v1, v58\n"
      "v_mul_f32 v59, v1, v59\n"
      "v_mul_f32 v60, v1, v60\n"
      "v_mul_f32 v61, v1, v61\n"
      "v_mul_f32 v62, v1, v62\n"
      "v_mul_f32 v63, v1, v63\n"
      "v_mul_f32 v64, v1, v64\n"
      "v_mul_f32 v65, v1, v65\n"
      "v_mul_f32 v66, v1, v66\n"
      "v_mul_f32 v67, v1, v67\n"
      "v_mul_f32 v68, v1, v68\n"
      "v_mul_f32 v69, v1, v69\n"
      "v_mul_f32 v70, v1, v70\n"
      "v_mul_f32 v71, v1, v71\n"
      "v_mul_f32 v72, v1, v72\n"
      "v_mul_f32 v73, v1, v73\n"
      "v_mul_f32 v74, v1, v74\n"
      "v_mul_f32 v75, v1, v75\n"
      "v_mul_f32 v76, v1, v76\n"
      "v_mul_f32 v77, v1, v77\n"
      "v_mul_f32 v78, v1, v78\n"
      "v_mul_f32 v79, v1, v79\n"
      "v_mul_f32 v80, v1, v80\n"
      "v_mul_f32 v81, v1, v81\n"
      "v_mul_f32 v82, v1, v82\n"
      "v_mul_f32 v83, v1, v83\n"
      "v_add_u32 v1, %[sd], v0\n"
      "v_add_u32 v2, %[kvd], v0\n"
      "s_setprio 0\n"
      "s_waitcnt vmcnt(16)\n"
      "s_barrier\n"
      "s_add_u32 m0, %[kvd], 0\n"
      "s_add_u32 %[t0], %[sp], 40960\n"
      "buffer_load_dwordx4 v0, %[rs], %[t0] offen offset:0 sc1 lds\n"
      "buffer_load_dwordx4 v0, %[rs], %[t0] offen offset:1024 sc1 lds\n"
      "buffer_load_dwordx4 v0, %[rs], %[t0] offen offset:2048 sc1 lds\n"
      "buffer_load_dwordx4 v0, %[rs], %[t0] offen offset:3072 sc1 lds\n"
      "s_add_u32 m0, %[kvd], 4096\n"
      "s_add_u32 %[t0], %[sp], 45056\n"
      "buffer_load_dwordx4 v0, %[rs], %[t0] offen offset:0 sc1 lds\n"
      "s_barrier\n"
      "s_add_u32 m0, %[kvd], 20992\n"
      "s_add_u32 %[t0], %[sp], 46080\n"
      "buffer_load_dwordx4 v0, %[rs], %[t0] offen offset:0 sc1 lds\n"
      "buffer_load_dwordx4 v0, %[rs], %[t0] offen offset:1024 sc1 lds\n"
      "buffer_load_dwordx4 v0, %[rs], %[t0] offen offset:2048 sc1 lds\n"
      "buffer_load_dwordx4 v0, %[rs], %[t0] offen offset:3072 sc1 lds\n"
      "s_add_u32 m0, %[kvd], 25088\n"
      "s_add_u32 %[t0], %[sp], 50176\n"
      "buffer_load_dwordx4 v0, %[rs], %[t0] offen offset:0 sc1 lds\n"
      "s_add_u32 %[t0], %[so], 0\n"
      "buffer_load_dwordx4 v[84:87], v0, %[rs], %[t0] offen sc1\n"
      "s_add_u32 %[t0], %[so], 1024\n"
      "buffer_load_dwordx4 v[88:91], v0, %[rs], %[t0] offen sc1\n"
      "s_add_u32 %[t0], %[so], 2048\n"
      "buffer_load_dwordx4 v[92:95], v0, %[rs], %[t0] offen sc1\n"
      "s_add_u32 %[t0], %[so], 3072\n"
      "buffer_load_dwordx4 v[96:99], v0, %[rs], %[t0] offen sc1\n"
      "s_add_u32 %[t0], %[so], 4096\n"
      "buffer_load_dwordx4 v[100:103], v0, %[rs], %[t0] offen sc1\n"
      "s_add_u32 %[t0], %[so], 5120\n"
      "buffer_load_dwordx4 v[104:107], v0, %[rs], %[t0] offen sc1\n"
      "s_add_u32 %[t0], %[so], 6144\n"
      "buffer_load_dwordx4 v[108:111], v0, %[rs], %[t0] offen sc1\n"
      "s_add_u32 %[t0], %[so], 7168\n"
      "buffer_load_dwordx4 v[112:115], v0, %[rs], %[t0] offen sc1\n"
      "s_add_u32 %[t0], %[so], 8192\n"
      "buffer_load_dwordx4 v[116:119], v0, %[rs], %[t0] offen sc1\n"
      "s_add_u32 %[t0], %[so], 9216\n"
      "buffer_load_dwordx4 v[120:123], v0, %[rs], %[t0] offen sc1\n"
      "s_mov_b32 %[sw], 0x40000\n"
      "s_waitcnt vmcnt(9)\n"
      "60:\n"
      "v_max3_u32 v3, v84, v85, v86\n"
      "v_max_u32 v3, v3, v87\n"
      "v_cmp_eq_u32 vcc, -1, v3\n"
      "s_cbranch_vccnz 20f\n"
      "30:\n"
      "v_mfma_f32_32x32x16_bf16 v[4:19], v[124:127], v[84:87], v[4:19]\n"
      "ds_read_b128 v[124:127], v1 offset:0\n"
      "v_mfma_f32_32x32x16_bf16 v[20:35], v[128:131], v[84:87], v[20:35]\n"
      "ds_read_b128 v[128:131], v1 offset:1024\n"
      "v_mfma_f32_32x32x16_bf16 v[36:51], v[132:135], v[84:87], v[36:51]\n"
      "ds_read_b128 v[132:135], v1 offset:2048\n"
      "v_mfma_f32_32x32x16_bf16 v[52:67], v[136:139], v[84:87], v[52:67]\n"
      "ds_read_b128 v[136:139], v1 offset:3072\n"
      "v_mfma_f32_32x32x16_bf16 v[68:83], v[140:143], v[84:87], v[68:83]\n"
      "ds_read_b128 v[140:143], v1 offset:4096\n"
      "s_waitcnt vmcnt(8)\n"
      "61:\n"
      "v_max3_u32 v3, v88, v89, v90\n"
      "v_max_u32 v3, v3, v91\n"
      "v_cmp_eq_u32 vcc, -1, v3\n"
      "s_cbranch_vccnz 21f\n"
      "31:\n"
      "v_mfma_f32_32x32x16_bf16 v[4:19], v[144:147], v[88:91], v[4:19]\n"
      "ds_read_b128 v[144:147], v1 offset:5120\n"
      "v_mfma_f32_32x32x16_bf16 v[20:35], v[148:151], v[88:91], v[20:35]\n"
      "ds_read_b128 v[148:151], v1 offset:6144\n"
      "v_mfma_f32_32x32x16_bf16 v[36:51], v[152:155], v[88:91], v[36:51]\n"
      "ds_read_b128 v[152:155], v1 offset:7168\n"
      "v_mfma_f32_32x32x16_bf16 v[52:67], v[156:159], v[88:91], v[52:67]\n"
      "ds_read_b128 v[156:159], v1 offset:8192\n"
      "v_mfma_f32_32x32x16_bf16 v[68:83], v[160:163], v[88:91], v[68:83]\n"
      "ds_read_b128 v[160:163], v1 offset:9216\n"
      "s_waitcnt vmcnt(7)\n"
      "62:\n"
      "v_max3_u32 v3, v92, v93, v94\n"
      "v_max_u32 v3, v3, v95\n"
      "v_cmp_eq_u32 vcc, -1, v3\n"
      "s_cbranch_vccnz 22f\n"
      "32:\n"
      "v_mfma_f32_32x32x16_bf16 v[4:19], v[164:167], v[92:95], v[4:19]\n"
      "ds_read_b128 v[164:167], v1 offset:10240\n"
      "s_waitcnt lgkmcnt(10)\n"
      "v_mfma_f32_32x32x16_bf16 v[20:35], v[124:127], v[92:95], v[20:35]\n"
      "ds_read_b128 v[124:127], v1 offset:11264\n"
      "s_waitcnt lgkmcnt(10)\n"
      "v_mfma_f32_32x32x16_bf16 v[36:51], v[128:131], v[92:95], v[36:51]\n"
      "ds_read_b128 v[128:131], v1 offset:49152\n"
      "s_waitcnt lgkmcnt(10)\n"
      "v_mfma_f32_32x32x16_bf16 v[52:67], v[132:135], v[92:95], v[52:67]\n"
      "ds_read_b128 v[132:135], v1 offset:50176\n"
      "s_waitcnt lgkmcnt(10)\n"
      "v_mfma_f32_32x32x16_bf16 v[68:83], v[136:139], v[92:95], v[68:83]\n"
      "ds_read_b128 v[136:139], v1 offset:51200\n"
      "s_waitcnt vmcnt(6)\n"
      "63:\n"
      "v_max3_u32 v3, v96, v97, v98\n"
      "v_max_u32 v3, v3, v99\n"
      "v_cmp_eq_u32 vcc, -1, v3\n"
      "s_cbranch_vccnz 23f\n"
      "33:\n"
      "s_waitcnt lgkmcnt(10)\n"
      "v_mfma_f32_32x32x16_bf16 v[4:19], v[140:143], v[96:99], v[4:19]\n"
      "ds_read_b128 v[140:143], v1 offset:52224\n"
      "s_waitcnt lgkmcnt(10)\n"
      "v_mfma_f32_32x32x16_bf16 v[20:35], v[144:147], v[96:99], v[20:35]\n"
      "ds_read_b128 v[144:147], v1 offset:53248\n"
      "s_waitcnt lgkmcnt(10)\n"
      "v_mfma_f32_32x32x16_bf16 v[36:51], v[148:151], v[96:99], v[36:51]\n"
      "ds_read_b128 v[148:151], v1 offset:54272\n"
      "s_waitcnt lgkmcnt(10)\n"
      "v_mfma_f32_32x32x16_bf16 v[52:67], v[152:155], v[96:99], v[52:67]\n"
      "ds_read_b128 v[152:155], v1 offset:55296\n"
      "s_waitcnt lgkmcnt(10)\n"
      "v_mfma_f32_32x32x16_bf16 v[68:83], v[156:159], v[96:99], v[68:83]\n"
      "ds_read_b128 v[156:159], v1 offset:56320\n"
      "s_waitcnt vmcnt(5)\n"
      "64:\n"
      "v_max3_u32 v3, v100, v101, v102\n"
      "v_max_u32 v3, v3, v103\n"
      "v_cmp_eq_u32 vcc, -1, v3\n"
      "s_cbranch_vccnz 24f\n"
      "34:\n"
      "s_waitcnt lgkmcnt(10)\n"
      "v_mfma_f32_32x32x16_bf16 v[4:19], v[160:163], v[100:103], v[4:19]\n"
      "ds_read_b128 v[160:163], v1 offset:57344\n"
      "s_waitcnt lgkmcnt(10)\n"
      "v_mfma_f32_32x32x16_bf16 v[20:35], v[164:167], v[100:103], v[20:35]\n"
      "ds_read_b128 v[164:167], v1 offset:58368\n"
      "s_waitcnt lgkmcnt(10)\n"
      "v_mfma_f32_32x32x16_bf16 v[36:51], v[124:127], v[100:103], v[36:51]\n"
      "ds_read_b128 v[124:127], v1 offset:59392\n"
      "s_waitcnt lgkmcnt(10)\n"
      "v_mfma_f32_32x32x16_bf16 v[52:67], v[128:131], v[100:103], v[52:67]\n"
      "ds_read_b128 v[128:131], v1 offset:60416\n"
      "s_waitcnt lgkmcnt(10)\n"
      "v_mfma_f32_32x32x16_bf16 v[68:83], v[132:135], v[100:103], v[68:83]\n"
      "ds_read_b128 v[132:135], v2 offset:41984\n"
      "s_waitcnt vmcnt(4)\n"
      "65:\n"
      "v_max3_u32 v3, v104, v105, v106\n"
      "v_max_u32 v3, v3, v107\n"
      "v_cmp_eq_u32 vcc, -1, v3\n"
      "s_cbranch_vccnz 25f\n"
      "35:\n"
      "s_waitcnt lgkmcnt(10)\n"
      "v_mfma_f32_32x32x16_bf16 v[4:19], v[136:139], v[104:107], v[4:19]\n"
      "ds_read_b128 v[136:139], v2 offset:43008\n"
      "s_waitcnt lgkmcnt(10)\n"
      "v_mfma_f32_32x32x16_bf16 v[20:35], v[140:143], v[104:107], v[20:35]\n"
      "ds_read_b128 v[140:143], v2 offset:44032\n"
      "s_waitcnt lgkmcnt(10)\n"
      "v_mfma_f32_32x32x16_bf16 v[36:51], v[144:147], v[104:107], v[36:51]\n"
      "ds_read_b128 v[144:147], v2 offset:45056\n"
      "s_waitcnt lgkmcnt(10)\n"
      "v_mfma_f32_32x32x16_bf16 v[52:67], v[148:151], v[104:107], v[52:67]\n"
      "ds_read_b128 v[148:151], v2 offset:46080\n"
      "s_waitcnt lgkmcnt(10)\n"
      "v_mfma_f32_32x32x16_bf16 v[68:83], v[152:155], v[104:107], v[68:83]\n"
      "ds_read_b128 v[152:155], v2 offset:0\n"
      "s_waitcnt vmcnt(3)\n"
      "66:\n"
      "v_max3_u32 v3, v108, v109, v110\n"
      "v_max_u32 v3, v3, v111\n"
      "v_cmp_eq_u32 vcc, -1, v3\n"
      "s_cbranch_vccnz 26f\n"
      "36:\n"
      "s_waitcnt lgkmcnt(10)\n"
      "v_mfma_f32_32x32x16_bf16 v[4:19], v[156:159], v[108:111], v[4:19]\n"
      "ds_read_b128 v[156:159], v2 offset:1024\n"
      "s_waitcnt lgkmcnt(10)\n"
      "v_mfma_f32_32x32x16_bf16 v[20:35], v[160:163], v[108:111], v[20:35]\n"
      "ds_read_b128 v[160:163], v2 offset:2048\n"
      "s_waitcnt lgkmcnt(10)\n"
      "v_mfma_f32_32x32x16_bf16 v[36:51], v[164:167], v[108:111], v[36:51]\n"
      "ds_read_b128 v[164:167], v2 offset:3072\n"
      "s_waitcnt lgkmcnt(10)\n"
      "v_mfma_f32_32x32x16_bf16 v[52:67], v[124:127], v[108:111], v[52:67]\n"
      "ds_read_b128 v[124:127], v2 offset:4096\n"
      "s_waitcnt lgkmcnt(10)\n"
      "v_mfma_f32_32x32x16_bf16 v[68:83], v[128:131], v[108:111], v[68:83]\n"
      "ds_read_b128 v[128:131], v2 offset:20992\n"
      "s_waitcnt vmcnt(2)\n"
      "67:\n"
      "v_max3_u32 v3, v112, v113, v114\n"
      "v_max_u32 v3, v3, v115\n"
      "v_cmp_eq_u32 vcc, -1, v3\n"
      "s_cbranch_vccnz 27f\n"
      "37:\n"
      "s_waitcnt lgkmcnt(10)\n"
      "v_mfma_f32_32x32x16_bf16 v[4:19], v[132:135], v[112:115], v[4:19]\n"
      "ds_read_b128 v[132:135], v2 offset:22016\n"
      "s_waitcnt lgkmcnt(10)\n"
      "v_mfma_f32_32x32x16_bf16 v[20:35], v[136:139], v[112:115], v[20:35]\n"
      "ds_read_b128 v[136:139], v2 offset:23040\n"
      "s_waitcnt lgkmcnt(10)\n"
      "v_mfma_f32_32x32x16_bf16 v[36:51], v[140:143], v[112:115], v[36:51]\n"
      "ds_read_b128 v[140:143], v2 offset:24064\n"
      "s_waitcnt lgkmcnt(10)\n"
      "v_mfma_f32_32x32x16_bf16 v[52:67], v[144:147], v[112:115], v[52:67]\n"
      "ds_read_b128 v[144:147], v2 offset:25088\n"
      "s_waitcnt lgkmcnt(10)\n"
      "v_mfma_f32_32x32x16_bf16 v[68:83], v[148:151], v[112:115], v[68:83]\n"
      "s_waitcnt vmcnt(1)\n"
      "68:\n"
      "v_max3_u32 v3, v116, v117, v118\n"
      "v_max_u32 v3, v3, v119\n"
      "v_cmp_eq_u32 vcc, -1, v3\n"
      "s_cbranch_vccnz 28f\n"
      "38:\n"
      "s_waitcnt lgkmcnt(9)\n"
      "v_mfma_f32_32x32x16_bf16 v[4:19], v[152:155], v[116:119], v[4:19]\n"
      "s_waitcnt lgkmcnt(8)\n"
      "v_mfma_f32_32x32x16_bf16 v[20:35], v[156:159], v[116:119], v[20:35]\n"
      "s_waitcnt lgkmcnt(7)\n"
      "v_mfma_f32_32x32x16_bf16 v[36:51], v[160:163], v[116:119], v[36:51]\n"
      "s_waitcnt lgkmcnt(6)\n"
      "v_mfma_f32_32x32x16_bf16 v[52:67], v[164:167], v[116:119], v[52:67]\n"
      "s_waitcnt lgkmcnt(5)\n"
      "v_mfma_f32_32x32x16_bf16 v[68:83], v[124:127], v[116:119], v[68:83]\n"
      "s_waitcnt vmcnt(0)\n"
      "69:\n"
      "v_max3_u32 v3, v120, v121, v122\n"
      "v_max_u32 v3, v3, v123\n"
      "v_cmp_eq_u32 vcc, -1, v3\n"
      "s_cbranch_vccnz 29f\n"
      "39:\n"
      "s_waitcnt lgkmcnt(4)\n"
      "v_mfma_f32_32x32x16_bf16 v[4:19], v[128:131], v[120:123], v[4:19]\n"
      "s_sub_u32 m0, %[sd], 40960\n"
      "s_nop 6\n"
      "s_waitcnt lgkmcnt(3)\n"
      "v_mfma_f32_32x32x16_bf16 v[20:35], v[132:135], v[120:123], v[20:35]\n"
      "s_nop 3\n"
      "ds_write_addtid_b32 v4 offset:40960\n"
      "ds_write_addtid_b32 v5 offset:41216\n"
      "ds_write_addtid_b32 v6 offset:41472\n"
      "ds_write_addtid_b32 v7 offset:41728\n"
      "ds_write_addtid_b32 v8 offset:42048\n"
      "ds_write_addtid_b32 v9 offset:42304\n"
      "ds_write_addtid_b32 v10 offset:42560\n"
      "ds_write_addtid_b32 v11 offset:42816\n"
      "ds_write_addtid_b32 v12 offset:43072\n"
      "ds_write_addtid_b32 v13 offset:43328\n"
      "ds_write_addtid_b32 v14 offset:43584\n"
      "ds_write_addtid_b32 v15 offset:43840\n"
      "ds_write_addtid_b32 v16 offset:44160\n"
      "ds_write_addtid_b32 v17 offset:44416\n"
      "ds_write_addtid_b32 v18 offset:44672\n"
      "ds_write_addtid_b32 v19 offset:44928\n"
      "s_waitcnt lgkmcnt(14)\n"
      "v_mfma_f32_32x32x16_bf16 v[36:51], v[136:139], v[120:123], v[36:51]\n"
      "ds_write_addtid_b32 v20 offset:45184\n"
      "ds_write_addtid_b32 v21 offset:45440\n"
      "ds_write_addtid_b32 v22 offset:45696\n"
      "ds_write_addtid_b32 v23 offset:45952\n"
      "ds_write_addtid_b32 v24 offset:46272\n"
      "ds_write_addtid_b32 v25 offset:46528\n"
      "ds_write_addtid_b32 v26 offset:46784\n"
      "ds_write_addtid_b32 v27 offset:47040\n"
      "ds_write_addtid_b32 v28 offset:47296\n"
      "ds_write_addtid_b32 v29 offset:47552\n"
      "ds_write_addtid_b32 v30 offset:47808\n"
      "ds_write_addtid_b32 v31 offset:48064\n"
      "ds_write_addtid_b32 v32 offset:48384\n"
      "ds_write_addtid_b32 v33 offset:48640\n"
      "ds_write_addtid_b32 v34 offset:48896\n"
      "ds_write_addtid_b32 v35 offset:49152\n"
      "s_waitcnt lgkmcnt(14)\n"
      "v_mfma_f32_32x32x16_bf16 v[52:67], v[140:143], v[120:123], v[52:67]\n"
      "s_mov_b32 m0, %[kvd]\n"
      "s_nop 0\n"
      "ds_write_addtid_b32 v36 offset:0\n"
      "ds_write_addtid_b32 v37 offset:256\n"
      "ds_write_addtid_b32 v38 offset:512\n"
      "ds_write_addtid_b32 v39 offset:768\n"
      "ds_write_addtid_b32 v40 offset:1088\n"
      "ds_write_addtid_b32 v41 offset:1344\n"
      "ds_write_addtid_b32 v42 offset:1600\n"
      "ds_write_addtid_b32 v43 offset:1856\n"
      "ds_write_addtid_b32 v44 offset:2112\n"
      "ds_write_addtid_b32 v45 offset:2368\n"
      "ds_write_addtid_b32 v46 offset:2624\n"
      "ds_write_addtid_b32 v47 offset:2880\n"
      "ds_write_addtid_b32 v48 offset:3200\n"
      "ds_write_addtid_b32 v49 offset:3456\n"
      "ds_write_addtid_b32 v50 offset:3712\n"
      "ds_write_addtid_b32 v51 offset:3968\n"
      "s_waitcnt lgkmcnt(14)\n"
      "v_mfma_f32_32x32x16_bf16 v[68:83], v[144:147], v[120:123], v[68:83]\n"
      "ds_write_addtid_b32 v52 offset:20992\n"
      "ds_write_addtid_b32 v53 offset:21248\n"
      "ds_write_addtid_b32 v54 offset:21504\n"
      "ds_write_addtid_b32 v55 offset:21760\n"
      "ds_write_addtid_b32 v56 offset:22080\n"
      "ds_write_addtid_b32 v57 offset:22336\n"
      "ds_write_addtid_b32 v58 offset:22592\n"
      "ds_write_addtid_b32 v59 offset:22848\n"
      "ds_write_addtid_b32 v60 offset:23104\n"
      "ds_write_addtid_b32 v61 offset:23360\n"
      "ds_write_addtid_b32 v62 offset:23616\n"
      "ds_write_addtid_b32 v63 offset:23872\n"
      "ds_write_addtid_b32 v64 offset:24192\n"
      "ds_write_addtid_b32 v65 offset:24448\n"
      "ds_write_addtid_b32 v66 offset:24704\n"
      "ds_write_addtid_b32 v67 offset:24960\n"
      "ds_write_addtid_b32 v68 offset:41984\n"
      "ds_write_addtid_b32 v69 offset:42240\n"
      "ds_write_addtid_b32 v70 offset:42496\n"
      "ds_write_addtid_b32 v71 offset:42752\n"
      "ds_write_addtid_b32 v72 offset:43072\n"
      "ds_write_addtid_b32 v73 offset:43328\n"
      "ds_write_addtid_b32 v74 offset:43584\n"
      "ds_write_addtid_b32 v75 offset:43840\n"
      "ds_write_addtid_b32 v76 offset:44096\n"
      "ds_write_addtid_b32 v77 offset:44352\n"
      "ds_write_addtid_b32 v78 offset:44608\n"
      "ds_write_addtid_b32 v79 offset:44864\n"
      "ds_write_addtid_b32 v80 offset:45184\n"
      "ds_write_addtid_b32 v81 offset:45440\n"
      "ds_write_addtid_b32 v82 offset:45696\n"
      "ds_write_addtid_b32 v83 offset:45952\n"
      "s_branch 40f\n"
      "20:\n"
      "s_mov_b32 %[t1], 0\n"
      "s_branch 70f\n"
      "21:\n"
      "s_mov_b32 %[t1], 1\n"
      "s_branch 70f\n"
      "22:\n"
      "s_mov_b32 %[t1], 2\n"
      "s_branch 70f\n"
      "23:\n"
      "s_mov_b32 %[t1], 3\n"
      "s_branch 70f\n"
      "24:\n"
      "s_mov_b32 %[t1], 4\n"
      "s_branch 70f\n"
      "25:\n"
      "s_mov_b32 %[t1], 5\n"
      "s_branch 70f\n"
      "26:\n"
      "s_mov_b32 %[t1], 6\n"
      "s_branch 70f\n"
      "27:\n"
      "s_mov_b32 %[t1], 7\n"
      "s_branch 70f\n"
      "28:\n"
      "s_mov_b32 %[t1], 8\n"
      "s_branch 70f\n"
      "29:\n"
      "s_mov_b32 %[t1], 9\n"
      "70:\n"
      "s_waitcnt vmcnt(0)\n"
      "s_sleep 1\n"
      "v_max3_u32 v3, v84, v85, v86\n"
      "v_max_u32 v3, v3, v87\n"
      "v_cmp_eq_u32 vcc, -1, v3\n"
      "s_cbranch_vccz 1f\n"
      "s_add_u32 %[t0], %[so], 0\n"
      "buffer_load_dwordx4 v[84:87], v0, %[rs], %[t0] offen sc1\n"
      "1:\n"
      "v_max3_u32 v3, v88, v89, v90\n"
      "v_max_u32 v3, v3, v91\n"
      "v_cmp_eq_u32 vcc, -1, v3\n"
      "s_cbranch_vccz 1f\n"
      "s_add_u32 %[t0], %[so], 1024\n"
      "buffer_load_dwordx4 v[88:91], v0, %[rs], %[t0] offen sc1\n"
      "1:\n"
      "v_max3_u32 v3, v92, v93, v94\n"
      "v_max_u32 v3, v3, v95\n"
      "v_cmp_eq_u32 vcc, -1, v3\n"
      "s_cbranch_vccz 1f\n"
      "s_add_u32 %[t0], %[so], 2048\n"
      "buffer_load_dwordx4 v[92:95], v0, %[rs], %[t0] offen sc1\n"
      "1:\n"
      "v_max3_u32 v3, v96, v97, v98\n"
      "v_max_u32 v3, v3, v99\n"
      "v_cmp_eq_u32 vcc, -1, v3\n"
      "s_cbranch_vccz 1f\n"
      "s_add_u32 %[t0], %[so], 3072\n"
      "buffer_load_dwordx4 v[96:99], v0, %[rs], %[t0] offen sc1\n"
      "1:\n"
      "v_max3_u32 v3, v100, v101, v102\n"
      "v_max_u32 v3, v3, v103\n"
      "v_cmp_eq_u32 vcc, -1, v3\n"
      "s_cbranch_vccz 1f\n"
      "s_add_u32 %[t0], %[so], 4096\n"
      "buffer_load_dwordx4 v[100:103], v0, %[rs], %[t0] offen sc1\n"
      "1:\n"
      "v_max3_u32 v3, v104, v105, v106\n"
      "v_max_u32 v3, v3, v107\n"
      "v_cmp_eq_u32 vcc, -1, v3\n"
      "s_cbranch_vccz 1f\n"
      "s_add_u32 %[t0], %[so], 5120\n"
      "buffer_load_dwordx4 v[104:107], v0, %[rs], %[t0] offen sc1\n"
      "1:\n"
      "v_max3_u32 v3, v108, v109, v110\n"
      "v_max_u32 v3, v3, v111\n"
      "v_cmp_eq_u32 vcc, -1, v3\n"
      "s_cbranch_vccz 1f\n"
      "s_add_u32 %[t0], %[so], 6144\n"
      "buffer_load_dwordx4 v[108:111], v0, %[rs], %[t0] offen sc1\n"
      "1:\n"
      "v_max3_u32 v3, v112, v113, v114\n"
      "v_max_u32 v3, v3, v115\n"
      "v_cmp_eq_u32 vcc, -1, v3\n"
      "s_cbranch_vccz 1f\n"
      "s_add_u32 %[t0], %[so], 7168\n"
      "buffer_load_dwordx4 v[112:115], v0, %[rs], %[t0] offen sc1\n"
      "1:\n"
      "v_max3_u32 v3, v116, v117, v118\n"
      "v_max_u32 v3, v3, v119\n"
      "v_cmp_eq_u32 vcc, -1, v3\n"
      "s_cbranch_vccz 1f\n"
      "s_add_u32 %[t0], %[so], 8192\n"
      "buffer_load_dwordx4 v[116:119], v0, %[rs], %[t0] offen sc1\n"
      "1:\n"
      "v_max3_u32 v3, v120, v121, v122\n"
      "v_max_u32 v3, v3, v123\n"
      "v_cmp_eq_u32 vcc, -1, v3\n"
      "s_cbranch_vccz 1f\n"
      "s_add_u32 %[t0], %[so], 9216\n"
      "buffer_load_dwordx4 v[120:123], v0, %[rs], %[t0] offen sc1\n"
      "1:\n"
      "71:\n"
      "s_waitcnt vmcnt(0)\n"
      "s_sub_u32 %[sw], %[sw], 1\n"
      "s_cbranch_scc1 72f\n"
      "s_cmp_eq_u32 %[t1], 0\n"
      "s_cbranch_scc1 60b\n"
      "s_cmp_eq_u32 %[t1], 1\n"
      "s_cbranch_scc1 61b\n"
      "s_cmp_eq_u32 %[t1], 2\n"
      "s_cbranch_scc1 62b\n"
      "s_cmp_eq_u32 %[t1], 3\n"
      "s_cbranch_scc1 63b\n"
      "s_cmp_eq_u32 %[t1], 4\n"
      "s_cbranch_scc1 64b\n"
      "s_cmp_eq_u32 %[t1], 5\n"
      "s_cbranch_scc1 65b\n"
      "s_cmp_eq_u32 %[t1], 6\n"
      "s_cbranch_scc1 66b\n"
      "s_cmp_eq_u32 %[t1], 7\n"
      "s_cbranch_scc1 67b\n"
      "s_cmp_eq_u32 %[t1], 8\n"
      "s_cbranch_scc1 68b\n"
      "s_branch 69b\n"
      "72:\n"
      "s_mov_b32 %[sw], 0\n"
      "s_cmp_eq_u32 %[t1], 0\n"
      "s_cbranch_scc1 30b\n"
      "s_cmp_eq_u32 %[t1], 1\n"
      "s_cbranch_scc1 31b\n"
      "s_cmp_eq_u32 %[t1], 2\n"
      "s_cbranch_scc1 32b\n"
      "s_cmp_eq_u32 %[t1], 3\n"
      "s_cbranch_scc1 33b\n"
      "s_cmp_eq_u32 %[t1], 4\n"
      "s_cbranch_scc1 34b\n"
      "s_cmp_eq_u32 %[t1], 5\n"
      "s_cbranch_scc1 35b\n"
      "s_cmp_eq_u32 %[t1], 6\n"
      "s_cbranch_scc1 36b\n"
      "s_cmp_eq_u32 %[t1], 7\n"
      "s_cbranch_scc1 37b\n"
      "s_cmp_eq_u32 %[t1], 8\n"
      "s_cbranch_scc1 38b\n"
      "s_branch 39b\n"
      "40:\n"
      : "=&{v[4:19]}"(acc[0]), "=&{v[20:35]}"(acc[1]), "=&{v[36:51]}"(acc[2]), "=&{v[52:67]}"(acc[3]), "=&{v[68:83]}"(acc[4]),
        "+{v0}"(l16), "+s"(ro), [sh] "+s"(sh), [sw] "+s"(sw), [t0] "=&s"(t0), [t1] "=&s"(t1)
      : [rkv] "s"(rkv), [rs] "s"(rs), [rb] "s"(rb2), [kvd] "s"(kvd), [kvs] "s"(kvs), [sg] "s"(sg), [sd] "s"(sd), [sx] "s"(sm + AT_X),
        [sp] "s"(sp), [so] "s"(so), [tl] "s"(tl), [th] "s"(th), [sf] "s"(sf), [pf] "s"(pf), [s3] "s"(s3), [f2] "n"(WS_F2), [f3] "n"(WS_F3), [ep] "n"(WS_EP)
      : "v1", "v2", "v3", "v84", "v85", "v86", "v87", "v88", "v89", "v90", "v91", "v92", "v93", "v94", "v95", "v96", "v97", "v98", "v99", "v100", "v101", "v102", "v103", "v104", "v105", "v106", "v107", "v108", "v109", "v110", "v111", "v112", "v113", "v114", "v115", "v116", "v117", "v118", "v119", "v120", "v121", "v122", "v123", "v124", "v125", "v126", "v127", "v128", "v129", "v130", "v131", "v132", "v133", "v134", "v135", "v136", "v137", "v138", "v139", "v140", "v141", "v142", "v143", "v144", "v145", "v146", "v147", "v148", "v149", "v150", "v151", "v152", "v153", "v154", "v155", "v156", "v157", "v158", "v159", "v160", "v161", "v162", "v163", "v164", "v165", "v166", "v167", "m0", "scc", "vcc", "memory");
    WAIT_LGKM0();
    bar();
    finish(sm, w, l16 >> 4, ro);
    return;
  }
  const int tok = lane & 15, kg = lane >> 4;
  const u32 xl = tok * 128 + ((kg ^ (tok >> 1)) << 4);
  auto wolds = [&](int i) { return sm + K1_RING + (i >> 1) * K1_SLOT + (i & 1) * 8192 + wid * 2048; };
  if (spare && wid < 4) {
    const rsrc_t rwo = mkrsrc(wo, DM * DM * 2);
    const int hh = (lane >> 5) & 1, r = (lane >> 3) & 3;
#pragma unroll
    for (int i = 0; i < 7; ++i) {
      const u32 u = rb * 25 + wid + 4 * i, st = u / 10, cu = u - st * 10, w4 = st / 10, e = st - w4 * 10;
      if (i == 6 && wid) break;
      const u32 r0 = e < 8 ? 20 * (8 * w4 + e) + 8 * hh + r : 20 * (8 * w4 + 4 * (e - 8) + hh) + 16 + r;
      wunit_issue(rwo, r0, r0 + (e < 8 ? 4 : 40), cu, wolds(i));
    }
  }
  const int hf = lane >> 5, l32 = lane & 31;
  u32x4 dv[6];
  u32 da[6], ns = 0;
#pragma unroll
  for (int i = 0; i < 6; ++i) { dv[i] = u32x4{~0u, ~0u, ~0u, ~0u}; da[i] = WS_O; }
  if (wid >= 4) {
    const int a = wid - 4;
    const u32x2 prev = __builtin_amdgcn_raw_buffer_load_b64(rs, 0, WS_EP, 16);
    auto late = [&](int s) {
      if (s == F1AT + 1) wait_vm<0>();
      bar();
      if (s == F1AT + 1 && a == 0) {
        __builtin_amdgcn_s_sleep(2);
        const u32x2 tag = calltag(prev);
        if (lane == 0) __builtin_amdgcn_raw_buffer_store_b64(tag, rs, 0, WS_F1 + b * 8, 16);
      }
      if (s == 19 && a < 2 && lane < 52) {
        const u32 c = a ? 0x3f803f80u : 0u;
        *(LDSP(u32x4))(sm + (lane >> 2) * AT_PITCH + a * 64 + (lane & 3) * 16) = u32x4{c, c, c, c};
      }
    };
    const u32 u = (rb * 15 + cb) * 4 + a, st = u / 10, cu = u - st * 10;
    const u32x4 sent = {~0u, ~0u, ~0u, ~0u};
    auto mark = [&]() {
#pragma unroll
      for (int i = 0; i < 3; ++i)
        if (i < 2 || a < 2) st16(rs, sent, lane * 16, WS_O + (b * 10 + 4 * i + a) * 1024);
    };
    auto w2load = [&]() { wunit_issue(mkrsrc(w2, FF * DM * 2), 16 * st + (lane >> 3), 16 * st + 8 + (lane >> 3), cu, sm + K1_AUX + a * 2048); };
    if (spare) {
      bf16x8 gf[20];
#pragma unroll
      for (int i = 0; i < 20; ++i) gf[i] = *(const bf16x8*)(gw + 64 * kst(i >> 1) + 32 * (i & 1) + 8 * kg);
      const bool res = (a >> 1) == half;
      const u32 xa = sm + (res ? 0 : K1_RING + 16384) + (a & 1) * 4096 + xl;
      f32x4 g0 = {}, g1 = {};
#pragma unroll
      for (int i = 0; i < 20; ++i) asm volatile("" : "+v"(gf[i]));
      bar();
#pragma unroll
      for (int s = 0; s < 10; ++s) {
        bar();
        if (s == 0) mark();
        const u32 p = xa + (res ? s * 8192 : (s % 3) * K1_SLOT);
        const bf16x8 x00 = lds128(p), x01 = lds128(p ^ 64), x10 = lds128(p + 2048), x11 = lds128((p ^ 64) + 2048);
        g0 = MFMA16(gf[2 * s + 1], x01, MFMA16(gf[2 * s], x00, g0));
        g1 = MFMA16(gf[2 * s + 1], x11, MFMA16(gf[2 * s], x10, g1));
        WAIT_LGKM0();
      }
      const float gv = 1.0f / (1.0f + __expf(-(kg & 1 ? g1[0] : g0[0])));
      if (lane < 32) __builtin_amdgcn_raw_buffer_store_b32(__float_as_uint(gv), rs, lane * 4, WS_G + (rb * 128 + a * 32) * 4, SC1);
#pragma unroll
      for (int s = 10; s < 21; ++s) late(s);
      WAIT_LGKM0();
      bar();
    } else {
      u32x4 wf[2];
      bar();
#pragma unroll
      for (int s = 0; s < 21; ++s) {
        late(s);
        if (s == 0) { mark(); w2load(); }
        if (s == W2RD) {
          wait_vm<0>();
#pragma unroll
          for (int f = 0; f < 2; ++f) { wf[f] = wunit_read(f, sm + K1_AUX + a * 2048); asm volatile("" : "+v"(wf[f])); }
        }
        if (s == W2ST) {
#pragma unroll
          for (int f = 0; f < 2; ++f) wunit_store(rs, WS_W2T, 96, st, cu, f, wf[f]);
        }
      }
      wait_vm<0>();
      WAIT_LGKM0();
      bar();
    }
    asm volatile("" :: "v"(touch));
  } else {
  const int r = (lane >> 2) & 3, cg = lane & 3;
  const u32 sg = (r >> 1) + 2 * (kg & 1);
  const int wm = wid >> 1, wn = wid & 1, cq = cb < 5 ? cb : cb - 5;
  u32 tab[4];
  u32x4 tq;
  asm volatile("s_load_dwordx4 %0, %1, %2" : "=s"(tq) : "s"(QKTAB[0]), "s"(rfl(cb < 10 ? cq * 32 + wn * 16 : 0)));
  f32x4 bias[3];
  u32x2 bb[3];
#pragma unroll
  for (int n = 0; n < 3; ++n) bb[n] = *(const u32x2*)(b1 + fb * 192 + (3 * wid + n) * 16 + 4 * kg);
  asm volatile("s_waitcnt lgkmcnt(0)" : "+s"(tq));
  u32 wq[4];
#pragma unroll
  for (int n = 0; n < 4; ++n) {
    tab[n] = cb < 10 ? tq[n] : 0x03020100u + 0x04040404u * (4 * wn + n);
    const u32 G = (tab[n] >> (8 * cg)) & 31;
    wq[n] = sm + K1_RING + kg * 4096 + (G >> 4) * 1024 + r * 128 + ((((G >> 2) & 3) ^ sg) << 5) + (G & 3) * 8;
  }
  const bool isq = cb < 5;
  u32 ea[5];
  if (cb < 10) {
    const u32 pos = isq ? l32 : (l32 & 3) | ((l32 & 12) << 1) | ((l32 & 16) >> 2);
    const u32 hs = isq ? 64 * Q_TILE : 32 * KV_PAIR;
#pragma unroll
    for (int n = 0; n < 5; ++n) {
      const u32 G = (tab[n < 4 ? n : 3] >> (16 * hf + (n < 4 ? 0 : 8))) & 31;
      const u32 g = 32 * cq + G, h = (g * 205) >> 10, gi = g - 5 * h;
      const u32 rk = h == 19 ? (gi ? gi - 1 : 4) : (h == 25 && gi >= 2 ? (gi == 2 ? 4 : gi - 1) : gi);
      ea[n] = h * hs + (rk < 4 ? (rk >> 1) * 512 + pos * 16 + (rk & 1) * 8 : 1024 + pos * 8);
    }
  } else {
#pragma unroll
    for (int n = 0; n < 4; ++n) {
      const u32 c = (cb - 10) * 128 + (4 * wn + n) * 16 + tok, h = (c * 3277) >> 16, d = c - 20 * h;
      ea[n] = h * (32 * KV_PAIR) + (d * 64 + ((kg ^ ((d >> 2) & 3)) << 4));
    }
    ea[4] = 0;
  }
#pragma unroll
  for (int n = 0; n < 5; ++n) asm volatile("" : "+v"(ea[n]), "+v"(wq[n & 3]));
#pragma unroll
  for (int n = 0; n < 2; ++n) {
    bias[n] = f32x4{bflo(bb[n][0]), bfhi(bb[n][0]), bflo(bb[n][1]), bfhi(bb[n][1])};
    asm volatile("" : "+v"(bias[n]));
  }
  asm volatile("" : "+v"(bb[2]));
  bar();
  if (spare) {
    bar();
    wait_vm<0>();
#pragma unroll
    for (int i = 0; i < 7; ++i) {
      const u32 u = rb * 25 + wid + 4 * i, st = u / 10, cu = u - st * 10;
      if (i == 6 && wid) break;
#pragma unroll
      for (int f = 0; f < 2; ++f) wunit_store(rs, WS_WOT, 40, st, cu, f, wunit_read(f, wolds(i)));
    }
#pragma unroll
    for (int s = 1; s < 11; ++s) bar();
  } else {
    const bool res = wm == half;
    u32 xa0 = sm + (res ? 0 : K1_RING + 16384) + xl, xa1 = xa0 ^ 64;
    const u32 d0 = res ? 8192 : K1_SLOT, d2 = res ? 8192 : -2 * K1_SLOT;
    f32x16 acc[4];
    if (cb < 10) {
      asm volatile(
      "s_barrier\n"
      "ds_read_b64_tr_b16 v[32:33], %[wa0] offset:0\n"
      "ds_read_b64_tr_b16 v[34:35], %[wa0] offset:512\n"
      "ds_read_b128 v[128:131], %[xa0] offset:0\n"
      "ds_read_b128 v[132:135], %[xa0] offset:2048\n"
      "ds_read_b128 v[136:139], %[xa0] offset:4096\n"
      "ds_read_b128 v[140:143], %[xa0] offset:6144\n"
      "ds_read_b64_tr_b16 v[36:37], %[wa1] offset:0\n"
      "ds_read_b64_tr_b16 v[38:39], %[wa1] offset:512\n"
      "ds_read_b64_tr_b16 v[40:41], %[wa2] offset:0\n"
      "ds_read_b64_tr_b16 v[42:43], %[wa2] offset:512\n"
      "ds_read_b64_tr_b16 v[44:45], %[wa3] offset:0\n"
      "ds_read_b64_tr_b16 v[46:47], %[wa3] offset:512\n"
      "v_add_u32 %[xa0], %[d0], %[xa0]\n"
      "s_waitcnt lgkmcnt(9)\n"
      "v_mfma_f32_16x16x32_bf16 v[64:67], v[32:35], v[128:131], 0\n"
      "ds_read_b64_tr_b16 v[48:49], %[wa0] offset:2048\n"
      "s_waitcnt lgkmcnt(9)\n"
      "v_mfma_f32_16x16x32_bf16 v[68:71], v[32:35], v[132:135], 0\n"
      "ds_read_b64_tr_b16 v[50:51], %[wa0] offset:2560\n"
      "s_waitcnt lgkmcnt(9)\n"
      "v_mfma_f32_16x16x32_bf16 v[72:75], v[32:35], v[136:139], 0\n"
      "ds_read_b128 v[144:147], %[xa1] offset:0\n"
      "s_waitcnt lgkmcnt(9)\n"
      "v_mfma_f32_16x16x32_bf16 v[76:79], v[32:35], v[140:143], 0\n"
      "ds_read_b128 v[148:151], %[xa1] offset:2048\n"
      "s_waitcnt lgkmcnt(8)\n"
      "v_mfma_f32_16x16x32_bf16 v[80:83], v[36:39], v[128:131], 0\n"
      "ds_read_b128 v[152:155], %[xa1] offset:4096\n"
      "v_mfma_f32_16x16x32_bf16 v[84:87], v[36:39], v[132:135], 0\n"
      "ds_read_b128 v[156:159], %[xa1] offset:6144\n"
      "v_add_u32 %[xa1], %[d0], %[xa1]\n"
      "v_mfma_f32_16x16x32_bf16 v[88:91], v[36:39], v[136:139], 0\n"
      "ds_read_b64_tr_b16 v[52:53], %[wa1] offset:2048\n"
      "v_mfma_f32_16x16x32_bf16 v[92:95], v[36:39], v[140:143], 0\n"
      "ds_read_b64_tr_b16 v[54:55], %[wa1] offset:2560\n"
      "s_waitcnt lgkmcnt(10)\n"
      "v_mfma_f32_16x16x32_bf16 v[96:99], v[40:43], v[128:131], 0\n"
      "ds_read_b64_tr_b16 v[56:57], %[wa2] offset:2048\n"
      "v_mfma_f32_16x16x32_bf16 v[100:103], v[40:43], v[132:135], 0\n"
      "ds_read_b64_tr_b16 v[58:59], %[wa2] offset:2560\n"
      "v_mfma_f32_16x16x32_bf16 v[104:107], v[40:43], v[136:139], 0\n"
      "ds_read_b64_tr_b16 v[60:61], %[wa3] offset:2048\n"
      "v_mfma_f32_16x16x32_bf16 v[108:111], v[40:43], v[140:143], 0\n"
      "ds_read_b64_tr_b16 v[62:63], %[wa3] offset:2560\n"
      "s_waitcnt lgkmcnt(12)\n"
      "v_mfma_f32_16x16x32_bf16 v[112:115], v[44:47], v[128:131], 0\n"
      "v_mfma_f32_16x16x32_bf16 v[116:119], v[44:47], v[132:135], 0\n"
      "v_mfma_f32_16x16x32_bf16 v[120:123], v[44:47], v[136:139], 0\n"
      "v_mfma_f32_16x16x32_bf16 v[124:127], v[44:47], v[140:143], 0\n"
      "s_waitcnt lgkmcnt(9)\n"
      "v_mfma_f32_16x16x32_bf16 v[64:67], v[48:51], v[144:147], v[64:67]\n"
      "s_waitcnt lgkmcnt(8)\n"
      "v_mfma_f32_16x16x32_bf16 v[68:71], v[48:51], v[148:151], v[68:71]\n"
      "s_waitcnt lgkmcnt(7)\n"
      "v_mfma_f32_16x16x32_bf16 v[72:75], v[48:51], v[152:155], v[72:75]\n"
      "s_waitcnt lgkmcnt(6)\n"
      "v_mfma_f32_16x16x32_bf16 v[76:79], v[48:51], v[156:159], v[76:79]\n"
      "s_waitcnt lgkmcnt(4)\n"
      "s_waitcnt lgkmcnt(0)\n"
      "s_barrier\n"
      "v_mfma_f32_16x16x32_bf16 v[80:83], v[52:55], v[144:147], v[80:83]\n"
      "ds_read_b64_tr_b16 v[32:33], %[wa0] offset:24576\n"
      "v_mfma_f32_16x16x32_bf16 v[84:87], v[52:55], v[148:151], v[84:87]\n"
      "ds_read_b64_tr_b16 v[34:35], %[wa0] offset:25088\n"
      "v_mfma_f32_16x16x32_bf16 v[88:91], v[52:55], v[152:155], v[88:91]\n"
      "ds_read_b128 v[128:131], %[xa0] offset:0\n"
      "v_mfma_f32_16x16x32_bf16 v[92:95], v[52:55], v[156:159], v[92:95]\n"
      "ds_read_b128 v[132:135], %[xa0] offset:2048\n"
      "v_mfma_f32_16x16x32_bf16 v[96:99], v[56:59], v[144:147], v[96:99]\n"
      "ds_read_b128 v[136:139], %[xa0] offset:4096\n"
      "v_mfma_f32_16x16x32_bf16 v[100:103], v[56:59], v[148:151], v[100:103]\n"
      "ds_read_b128 v[140:143], %[xa0] offset:6144\n"
      "v_add_u32 %[xa0], %[d1], %[xa0]\n"
      "v_mfma_f32_16x16x32_bf16 v[104:107], v[56:59], v[152:155], v[104:107]\n"
      "ds_read_b64_tr_b16 v[36:37], %[wa1] offset:24576\n"
      "v_mfma_f32_16x16x32_bf16 v[108:111], v[56:59], v[156:159], v[108:111]\n"
      "ds_read_b64_tr_b16 v[38:39], %[wa1] offset:25088\n"
      "v_mfma_f32_16x16x32_bf16 v[112:115], v[60:63], v[144:147], v[112:115]\n"
      "ds_read_b64_tr_b16 v[40:41], %[wa2] offset:24576\n"
      "v_mfma_f32_16x16x32_bf16 v[116:119], v[60:63], v[148:151], v[116:119]\n"
      "ds_read_b64_tr_b16 v[42:43], %[wa2] offset:25088\n"
      "v_mfma_f32_16x16x32_bf16 v[120:123], v[60:63], v[152:155], v[120:123]\n"
      "ds_read_b64_tr_b16 v[44:45], %[wa3] offset:24576\n"
      "v_mfma_f32_16x16x32_bf16 v[124:127], v[60:63], v[156:159], v[124:127]\n"
      "ds_read_b64_tr_b16 v[46:47], %[wa3] offset:25088\n"
      "s_waitcnt lgkmcnt(9)\n"
      "v_mfma_f32_16x16x32_bf16 v[64:67], v[32:35], v[128:131], v[64:67]\n"
      "ds_read_b64_tr_b16 v[48:49], %[wa0] offset:26624\n"
      "s_waitcnt lgkmcnt(9)\n"
      "v_mfma_f32_16x16x32_bf16 v[68:71], v[32:35], v[132:135], v[68:71]\n"
      "ds_read_b64_tr_b16 v[50:51], %[wa0] offset:27136\n"
      "s_waitcnt lgkmcnt(9)\n"
      "v_mfma_f32_16x16x32_bf16 v[72:75], v[32:35], v[136:139], v[72:75]\n"
      "ds_read_b128 v[144:147], %[xa1] offset:0\n"
      "s_waitcnt lgkmcnt(9)\n"
      "v_mfma_f32_16x16x32_bf16 v[76:79], v[32:35], v[140:143], v[76:79]\n"
      "ds_read_b128 v[148:151], %[xa1] offset:2048\n"
      "s_waitcnt lgkmcnt(8)\n"
      "v_mfma_f32_16x16x32_bf16 v[80:83], v[36:39], v[128:131], v[80:83]\n"
      "ds_read_b128 v[152:155], %[xa1] offset:4096\n"
      "v_mfma_f32_16x16x32_bf16 v[84:87], v[36:39], v[132:135], v[84:87]\n"
      "ds_read_b128 v[156:159], %[xa1] offset:6144\n"
      "v_add_u32 %[xa1], %[d1], %[xa1]\n"
      "v_mfma_f32_16x16x32_bf16 v[88:91], v[36:39], v[136:139], v[88:91]\n"
      "ds_read_b64_tr_b16 v[52:53], %[wa1] offset:26624\n"
      "v_mfma_f32_16x16x32_bf16 v[92:95], v[36:39], v[140:143], v[92:95]\n"
      "ds_read_b64_tr_b16 v[54:55], %[wa1] offset:27136\n"
      "s_waitcnt lgkmcnt(10)\n"
      "v_mfma_f32_16x16x32_bf16 v[96:99], v[40:43], v[128:131], v[96:99]\n"
      "ds_read_b64_tr_b16 v[56:57], %[wa2] offset:26624\n"
      "v_mfma_f32_16x16x32_bf16 v[100:103], v[40:43], v[132:135], v[100:103]\n"
      "ds_read_b64_tr_b16 v[58:59], %[wa2] offset:27136\n"
      "v_mfma_f32_16x16x32_bf16 v[104:107], v[40:43], v[136:139], v[104:107]\n"
      "ds_read_b64_tr_b16 v[60:61], %[wa3] offset:26624\n"
      "v_mfma_f32_16x16x32_bf16 v[108:111], v[40:43], v[140:143], v[108:111]\n"
      "ds_read_b64_tr_b16 v[62:63], %[wa3] offset:27136\n"
      "s_waitcnt lgkmcnt(12)\n"
      "v_mfma_f32_16x16x32_bf16 v[112:115], v[44:47], v[128:131], v[112:115]\n"
      "v_mfma_f32_16x16x32_bf16 v[116:119], v[44:47], v[132:135], v[116:119]\n"
      "v_mfma_f32_16x16x32_bf16 v[120:123], v[44:47], v[136:139], v[120:123]\n"
      "v_mfma_f32_16x16x32_bf16 v[124:127], v[44:47], v[140:143], v[124:127]\n"
      "s_waitcnt lgkmcnt(9)\n"
      "v_mfma_f32_16x16x32_bf16 v[64:67], v[48:51], v[144:147], v[64:67]\n"
      "s_waitcnt lgkmcnt(8)\n"
      "v_mfma_f32_16x16x32_bf16 v[68:71], v[48:51], v[148:151], v[68:71]\n"
      "s_waitcnt lgkmcnt(7)\n"
      "v_mfma_f32_16x16x32_bf16 v[72:75], v[48:51], v[152:155], v[72:75]\n"
      "s_waitcnt lgkmcnt(6)\n"
      "v_mfma_f32_16x16x32_bf16 v[76:79], v[48:51], v[156:159], v[76:79]\n"
      "s_waitcnt lgkmcnt(4)\n"
      "s_waitcnt lgkmcnt(0)\n"
      "s_barrier\n"
      "v_mfma_f32_16x16x32_bf16 v[80:83], v[52:55], v[144:147], v[80:83]\n"
      "ds_read_b64_tr_b16 v[32:33], %[wa0] offset:49152\n"
      "v_mfma_f32_16x16x32_bf16 v[84:87], v[52:55], v[148:151], v[84:87]\n"
      "ds_read_b64_tr_b16 v[34:35], %[wa0] offset:49664\n"
      "v_mfma_f32_16x16x32_bf16 v[88:91], v[52:55], v[152:155], v[88:91]\n"
      "ds_read_b128 v[128:131], %[xa0] offset:0\n"
      "v_mfma_f32_16x16x32_bf16 v[92:95], v[52:55], v[156:159], v[92:95]\n"
      "ds_read_b128 v[132:135], %[xa0] offset:2048\n"
      "v_mfma_f32_16x16x32_bf16 v[96:99], v[56:59], v[144:147], v[96:99]\n"
      "ds_read_b128 v[136:139], %[xa0] offset:4096\n"
      "v_mfma_f32_16x16x32_bf16 v[100:103], v[56:59], v[148:151], v[100:103]\n"
      "ds_read_b128 v[140:143], %[xa0] offset:6144\n"
      "v_add_u32 %[xa0], %[d2], %[xa0]\n"
      "v_mfma_f32_16x16x32_bf16 v[104:107], v[56:59], v[152:155], v[104:107]\n"
      "ds_read_b64_tr_b16 v[36:37], %[wa1] offset:49152\n"
      "v_mfma_f32_16x16x32_bf16 v[108:111], v[56:59], v[156:159], v[108:111]\n"
      "ds_read_b64_tr_b16 v[38:39], %[wa1] offset:49664\n"
      "v_mfma_f32_16x16x32_bf16 v[112:115], v[60:63], v[144:147], v[112:115]\n"
      "ds_read_b64_tr_b16 v[40:41], %[wa2] offset:49152\n"
      "v_mfma_f32_16x16x32_bf16 v[116:119], v[60:63], v[148:151], v[116:119]\n"
      "ds_read_b64_tr_b16 v[42:43], %[wa2] offset:49664\n"
      "v_mfma_f32_16x16x32_bf16 v[120:123], v[60:63], v[152:155], v[120:123]\n"
      "ds_read_b64_tr_b16 v[44:45], %[wa3] offset:49152\n"
      "v_mfma_f32_16x16x32_bf16 v[124:127], v[60:63], v[156:159], v[124:127]\n"
      "ds_read_b64_tr_b16 v[46:47], %[wa3] offset:49664\n"
      "s_waitcnt lgkmcnt(9)\n"
      "v_mfma_f32_16x16x32_bf16 v[64:67], v[32:35], v[128:131], v[64:67]\n"
      "ds_read_b64_tr_b16 v[48:49], %[wa0] offset:51200\n"
      "s_waitcnt lgkmcnt(9)\n"
      "v_mfma_f32_16x16x32_bf16 v[68:71], v[32:35], v[132:135], v[68:71]\n"
      "ds_read_b64_tr_b16 v[50:51], %[wa0] offset:51712\n"
      "s_waitcnt lgkmcnt(9)\n"
      "v_mfma_f32_16x16x32_bf16 v[72:75], v[32:35], v[136:139], v[72:75]\n"
      "ds_read_b128 v[144:147], %[xa1] offset:0\n"
      "s_waitcnt lgkmcnt(9)\n"
      "v_mfma_f32_16x16x32_bf16 v[76:79], v[32:35], v[140:143], v[76:79]\n"
      "ds_read_b128 v[148:151], %[xa1] offset:2048\n"
      "s_waitcnt lgkmcnt(8)\n"
      "v_mfma_f32_16x16x32_bf16 v[80:83], v[36:39], v[128:131], v[80:83]\n"
      "ds_read_b128 v[152:155], %[xa1] offset:4096\n"
      "v_mfma_f32_16x16x32_bf16 v[84:87], v[36:39], v[132:135], v[84:87]\n"
      "ds_read_b128 v[156:159], %[xa1] offset:6144\n"
      "v_add_u32 %[xa1], %[d2], %[xa1]\n"
      "v_mfma_f32_16x16x32_bf16 v[88:91], v[36:39], v[136:139], v[88:91]\n"
      "ds_read_b64_tr_b16 v[52:53], %[wa1] offset:51200\n"
      "v_mfma_f32_16x16x32_bf16 v[92:95], v[36:39], v[140:143], v[92:95]\n"
      "ds_read_b64_tr_b16 v[54:55], %[wa1] offset:51712\n"
      "s_waitcnt lgkmcnt(10)\n"
      "v_mfma_f32_16x16x32_bf16 v[96:99], v[40:43], v[128:131], v[96:99]\n"
      "ds_read_b64_tr_b16 v[56:57], %[wa2] offset:51200\n"
      "v_mfma_f32_16x16x32_bf16 v[100:103], v[40:43], v[132:135], v[100:103]\n"
      "ds_read_b64_tr_b16 v[58:59], %[wa2] offset:51712\n"
      "v_mfma_f32_16x16x32_bf16 v[104:107], v[40:43], v[136:139], v[104:107]\n"
      "ds_read_b64_tr_b16 v[60:61], %[wa3] offset:51200\n"
      "v_mfma_f32_16x16x32_bf16 v[108:111], v[40:43], v[140:143], v[108:111]\n"
      "ds_read_b64_tr_b16 v[62:63], %[wa3] offset:51712\n"
      "s_waitcnt lgkmcnt(12)\n"
      "v_mfma_f32_16x16x32_bf16 v[112:115], v[44:47], v[128:131], v[112:115]\n"
      "v_mfma_f32_16x16x32_bf16 v[116:119], v[44:47], v[132:135], v[116:119]\n"
      "v_mfma_f32_16x16x32_bf16 v[120:123], v[44:47], v[136:139], v[120:123]\n"
      "v_mfma_f32_16x16x32_bf16 v[124:127], v[44:47], v[140:143], v[124:127]\n"
      "s_waitcnt lgkmcnt(9)\n"
      "v_mfma_f32_16x16x32_bf16 v[64:67], v[48:51], v[144:147], v[64:67]\n"
      "s_waitcnt lgkmcnt(8)\n"
      "v_mfma_f32_16x16x32_bf16 v[68:71], v[48:51], v[148:151], v[68:71]\n"
      "s_waitcnt lgkmcnt(7)\n"
      "v_mfma_f32_16x16x32_bf16 v[72:75], v[48:51], v[152:155], v[72:75]\n"
      "s_waitcnt lgkmcnt(6)\n"
      "v_mfma_f32_16x16x32_bf16 v[76:79], v[48:51], v[156:159], v[76:79]\n"
      "s_waitcnt lgkmcnt(4)\n"
      "s_waitcnt lgkmcnt(0)\n"
      "s_barrier\n"
      "v_mfma_f32_16x16x32_bf16 v[80:83], v[52:55], v[144:147], v[80:83]\n"
      "ds_read_b64_tr_b16 v[32:33], %[wa0] offset:0\n"
      "v_mfma_f32_16x16x32_bf16 v[84:87], v[52:55], v[148:151], v[84:87]\n"
      "ds_read_b64_tr_b16 v[34:35], %[wa0] offset:512\n"
      "v_mfma_f32_16x16x32_bf16 v[88:91], v[52:55], v[152:155], v[88:91]\n"
      "ds_read_b128 v[128:131], %[xa0] offset:0\n"
      "v_mfma_f32_16x16x32_bf16 v[92:95], v[52:55], v[156:159], v[92:95]\n"
      "ds_read_b128 v[132:135], %[xa0] offset:2048\n"
      "v_mfma_f32_16x16x32_bf16 v[96:99], v[56:59], v[144:147], v[96:99]\n"
      "ds_read_b128 v[136:139], %[xa0] offset:4096\n"
      "v_mfma_f32_16x16x32_bf16 v[100:103], v[56:59], v[148:151], v[100:103]\n"
      "ds_read_b128 v[140:143], %[xa0] offset:6144\n"
      "v_add_u32 %[xa0], %[d0], %[xa0]\n"
      "v_mfma_f32_16x16x32_bf16 v[104:107], v[56:59], v[152:155], v[104:107]\n"
      "ds_read_b64_tr_b16 v[36:37], %[wa1] offset:0\n"
      "v_mfma_f32_16x16x32_bf16 v[108:111], v[56:59], v[156:159], v[108:111]\n"
      "ds_read_b64_tr_b16 v[38:39], %[wa1] offset:512\n"
      "v_mfma_f32_16x16x32_bf16 v[112:115], v[60:63], v[144:147], v[112:115]\n"
      "ds_read_b64_tr_b16 v[40:41], %[wa2] offset:0\n"
      "v_mfma_f32_16x16x32_bf16 v[116:119], v[60:63], v[148:151], v[116:119]\n"
      "ds_read_b64_tr_b16 v[42:43], %[wa2] offset:512\n"
      "v_mfma_f32_16x16x32_bf16 v[120:123], v[60:63], v[152:155], v[120:123]\n"
      "ds_read_b64_tr_b16 v[44:45], %[wa3] offset:0\n"
      "v_mfma_f32_16x16x32_bf16 v[124:127], v[60:63], v[156:159], v[124:127]\n"
      "ds_read_b64_tr_b16 v[46:47], %[wa3] offset:512\n"
      "s_waitcnt lgkmcnt(9)\n"
      "v_mfma_f32_16x16x32_bf16 v[64:67], v[32:35], v[128:131], v[64:67]\n"
      "ds_read_b64_tr_b16 v[48:49], %[wa0] offset:2048\n"
      "s_waitcnt lgkmcnt(9)\n"
      "v_mfma_f32_16x16x32_bf16 v[68:71], v[32:35], v[132:135], v[68:71]\n"
      "ds_read_b64_tr_b16 v[50:51], %[wa0] offset:2560\n"
      "s_waitcnt lgkmcnt(9)\n"
      "v_mfma_f32_16x16x32_bf16 v[72:75], v[32:35], v[136:139], v[72:75]\n"
      "ds_read_b128 v[144:147], %[xa1] offset:0\n"
      "s_waitcnt lgkmcnt(9)\n"
      "v_mfma_f32_16x16x32_bf16 v[76:79], v[32:35], v[140:143], v[76:79]\n"
      "ds_read_b128 v[148:151], %[xa1] offset:2048\n"
      "s_waitcnt lgkmcnt(8)\n"
      "v_mfma_f32_16x16x32_bf16 v[80:83], v[36:39], v[128:131], v[80:83]\n"
      "ds_read_b128 v[152:155], %[xa1] offset:4096\n"
      "v_mfma_f32_16x16x32_bf16 v[84:87], v[36:39], v[132:135], v[84:87]\n"
      "ds_read_b128 v[156:159], %[xa1] offset:6144\n"
      "v_add_u32 %[xa1], %[d0], %[xa1]\n"
      "v_mfma_f32_16x16x32_bf16 v[88:91], v[36:39], v[136:139], v[88:91]\n"
      "ds_read_b64_tr_b16 v[52:53], %[wa1] offset:2048\n"
      "v_mfma_f32_16x16x32_bf16 v[92:95], v[36:39], v[140:143], v[92:95]\n"
      "ds_read_b64_tr_b16 v[54:55], %[wa1] offset:2560\n"
      "s_waitcnt lgkmcnt(10)\n"
      "v_mfma_f32_16x16x32_bf16 v[96:99], v[40:43], v[128:131], v[96:99]\n"
      "ds_read_b64_tr_b16 v[56:57], %[wa2] offset:2048\n"
      "v_mfma_f32_16x16x32_bf16 v[100:103], v[40:43], v[132:135], v[100:103]\n"
      "ds_read_b64_tr_b16 v[58:59], %[wa2] offset:2560\n"
      "v_mfma_f32_16x16x32_bf16 v[104:107], v[40:43], v[136:139], v[104:107]\n"
      "ds_read_b64_tr_b16 v[60:61], %[wa3] offset:2048\n"
      "v_mfma_f32_16x16x32_bf16 v[108:111], v[40:43], v[140:143], v[108:111]\n"
      "ds_read_b64_tr_b16 v[62:63], %[wa3] offset:2560\n"
      "s_waitcnt lgkmcnt(12)\n"
      "v_mfma_f32_16x16x32_bf16 v[112:115], v[44:47], v[128:131], v[112:115]\n"
      "v_mfma_f32_16x16x32_bf16 v[116:119], v[44:47], v[132:135], v[116:119]\n"
      "v_mfma_f32_16x16x32_bf16 v[120:123], v[44:47], v[136:139], v[120:123]\n"
      "v_mfma_f32_16x16x32_bf16 v[124:127], v[44:47], v[140:143], v[124:127]\n"
      "s_waitcnt lgkmcnt(9)\n"
      "v_mfma_f32_16x16x32_bf16 v[64:67], v[48:51], v[144:147], v[64:67]\n"
      "s_waitcnt lgkmcnt(8)\n"
      "v_mfma_f32_16x16x32_bf16 v[68:71], v[48:51], v[148:151], v[68:71]\n"
      "s_waitcnt lgkmcnt(7)\n"
      "v_mfma_f32_16x16x32_bf16 v[72:75], v[48:51], v[152:155], v[72:75]\n"
      "s_waitcnt lgkmcnt(6)\n"
      "v_mfma_f32_16x16x32_bf16 v[76:79], v[48:51], v[156:159], v[76:79]\n"
      "s_waitcnt lgkmcnt(4)\n"
      "s_waitcnt lgkmcnt(0)\n"
      "s_barrier\n"
      "v_mfma_f32_16x16x32_bf16 v[80:83], v[52:55], v[144:147], v[80:83]\n"
      "ds_read_b64_tr_b16 v[32:33], %[wa0] offset:24576\n"
      "v_mfma_f32_16x16x32_bf16 v[84:87], v[52:55], v[148:151], v[84:87]\n"
      "ds_read_b64_tr_b16 v[34:35], %[wa0] offset:25088\n"
      "v_mfma_f32_16x16x32_bf16 v[88:91], v[52:55], v[152:155], v[88:91]\n"
      "ds_read_b128 v[128:131], %[xa0] offset:0\n"
      "v_mfma_f32_16x16x32_bf16 v[92:95], v[52:55], v[156:159], v[92:95]\n"
      "ds_read_b128 v[132:135], %[xa0] offset:2048\n"
      "v_mfma_f32_16x16x32_bf16 v[96:99], v[56:59], v[144:147], v[96:99]\n"
      "ds_read_b128 v[136:139], %[xa0] offset:4096\n"
      "v_mfma_f32_16x16x32_bf16 v[100:103], v[56:59], v[148:151], v[100:103]\n"
      "ds_read_b128 v[140:143], %[xa0] offset:6144\n"
      "v_add_u32 %[xa0], %[d1], %[xa0]\n"
      "v_mfma_f32_16x16x32_bf16 v[104:107], v[56:59], v[152:155], v[104:107]\n"
      "ds_read_b64_tr_b16 v[36:37], %[wa1] offset:24576\n"
      "v_mfma_f32_16x16x32_bf16 v[108:111], v[56:59], v[156:159], v[108:111]\n"
      "ds_read_b64_tr_b16 v[38:39], %[wa1] offset:25088\n"
      "v_mfma_f32_16x16x32_bf16 v[112:115], v[60:63], v[144:147], v[112:115]\n"
      "ds_read_b64_tr_b16 v[40:41], %[wa2] offset:24576\n"
      "v_mfma_f32_16x16x32_bf16 v[116:119], v[60:63], v[148:151], v[116:119]\n"
      "ds_read_b64_tr_b16 v[42:43], %[wa2] offset:25088\n"
      "v_mfma_f32_16x16x32_bf16 v[120:123], v[60:63], v[152:155], v[120:123]\n"
      "ds_read_b64_tr_b16 v[44:45], %[wa3] offset:24576\n"
      "v_mfma_f32_16x16x32_bf16 v[124:127], v[60:63], v[156:159], v[124:127]\n"
      "ds_read_b64_tr_b16 v[46:47], %[wa3] offset:25088\n"
      "s_waitcnt lgkmcnt(9)\n"
      "v_mfma_f32_16x16x32_bf16 v[64:67], v[32:35], v[128:131], v[64:67]\n"
      "ds_read_b64_tr_b16 v[48:49], %[wa0] offset:26624\n"
      "s_waitcnt lgkmcnt(9)\n"
      "v_mfma_f32_16x16x32_bf16 v[68:71], v[32:35], v[132:135], v[68:71]\n"
      "ds_read_b64_tr_b16 v[50:51], %[wa0] offset:27136\n"
      "s_waitcnt lgkmcnt(9)\n"
      "v_mfma_f32_16x16x32_bf16 v[72:75], v[32:35], v[136:139], v[72:75]\n"
      "ds_read_b128 v[144:147], %[xa1] offset:0\n"
      "s_waitcnt lgkmcnt(9)\n"
      "v_mfma_f32_16x16x32_bf16 v[76:79], v[32:35], v[140:143], v[76:79]\n"
      "ds_read_b128 v[148:151], %[xa1] offset:2048\n"
      "s_waitcnt lgkmcnt(8)\n"
      "v_mfma_f32_16x16x32_bf16 v[80:83], v[36:39], v[128:131], v[80:83]\n"
      "ds_read_b128 v[152:155], %[xa1] offset:4096\n"
      "v_mfma_f32_16x16x32_bf16 v[84:87], v[36:39], v[132:135], v[84:87]\n"
      "ds_read_b128 v[156:159], %[xa1] offset:6144\n"
      "v_add_u32 %[xa1], %[d1], %[xa1]\n"
      "v_mfma_f32_16x16x32_bf16 v[88:91], v[36:39], v[136:139], v[88:91]\n"
      "ds_read_b64_tr_b16 v[52:53], %[wa1] offset:26624\n"
      "v_mfma_f32_16x16x32_bf16 v[92:95], v[36:39], v[140:143], v[92:95]\n"
      "ds_read_b64_tr_b16 v[54:55], %[wa1] offset:27136\n"
      "s_waitcnt lgkmcnt(10)\n"
      "v_mfma_f32_16x16x32_bf16 v[96:99], v[40:43], v[128:131], v[96:99]\n"
      "ds_read_b64_tr_b16 v[56:57], %[wa2] offset:26624\n"
      "v_mfma_f32_16x16x32_bf16 v[100:103], v[40:43], v[132:135], v[100:103]\n"
      "ds_read_b64_tr_b16 v[58:59], %[wa2] offset:27136\n"
      "v_mfma_f32_16x16x32_bf16 v[104:107], v[40:43], v[136:139], v[104:107]\n"
      "ds_read_b64_tr_b16 v[60:61], %[wa3] offset:26624\n"
      "v_mfma_f32_16x16x32_bf16 v[108:111], v[40:43], v[140:143], v[108:111]\n"
      "ds_read_b64_tr_b16 v[62:63], %[wa3] offset:27136\n"
      "s_waitcnt lgkmcnt(12)\n"
      "v_mfma_f32_16x16x32_bf16 v[112:115], v[44:47], v[128:131], v[112:115]\n"
      "v_mfma_f32_16x16x32_bf16 v[116:119], v[44:47], v[132:135], v[116:119]\n"
      "v_mfma_f32_16x16x32_bf16 v[120:123], v[44:47], v[136:139], v[120:123]\n"
      "v_mfma_f32_16x16x32_bf16 v[124:127], v[44:47], v[140:143], v[124:127]\n"
      "s_waitcnt lgkmcnt(9)\n"
      "v_mfma_f32_16x16x32_bf16 v[64:67], v[48:51], v[144:147], v[64:67]\n"
      "s_waitcnt lgkmcnt(8)\n"
      "v_mfma_f32_16x16x32_bf16 v[68:71], v[48:51], v[148:151], v[68:71]\n"
      "s_waitcnt lgkmcnt(7)\n"
      "v_mfma_f32_16x16x32_bf16 v[72:75], v[48:51], v[152:155], v[72:75]\n"
      "s_waitcnt lgkmcnt(6)\n"
      "v_mfma_f32_16x16x32_bf16 v[76:79], v[48:51], v[156:159], v[76:79]\n"
      "s_waitcnt lgkmcnt(4)\n"
      "s_waitcnt lgkmcnt(0)\n"
      "s_barrier\n"
      "v_mfma_f32_16x16x32_bf16 v[80:83], v[52:55], v[144:147], v[80:83]\n"
      "ds_read_b64_tr_b16 v[32:33], %[wa0] offset:49152\n"
      "v_mfma_f32_16x16x32_bf16 v[84:87], v[52:55], v[148:151], v[84:87]\n"
      "ds_read_b64_tr_b16 v[34:35], %[wa0] offset:49664\n"
      "v_mfma_f32_16x16x32_bf16 v[88:91], v[52:55], v[152:155], v[88:91]\n"
      "ds_read_b128 v[128:131], %[xa0] offset:0\n"
      "v_mfma_f32_16x16x32_bf16 v[92:95], v[52:55], v[156:159], v[92:95]\n"
      "ds_read_b128 v[132:135], %[xa0] offset:2048\n"
      "v_mfma_f32_16x16x32_bf16 v[96:99], v[56:59], v[144:147], v[96:99]\n"
      "ds_read_b128 v[136:139], %[xa0] offset:4096\n"
      "v_mfma_f32_16x16x32_bf16 v[100:103], v[56:59], v[148:151], v[100:103]\n"
      "ds_read_b128 v[140:143], %[xa0] offset:6144\n"
      "v_add_u32 %[xa0], %[d2], %[xa0]\n"
      "v_mfma_f32_16x16x32_bf16 v[104:107], v[56:59], v[152:155], v[104:107]\n"
      "ds_read_b64_tr_b16 v[36:37], %[wa1] offset:49152\n"
      "v_mfma_f32_16x16x32_bf16 v[108:111], v[56:59], v[156:159], v[108:111]\n"
      "ds_read_b64_tr_b16 v[38:39], %[wa1] offset:49664\n"
      "v_mfma_f32_16x16x32_bf16 v[112:115], v[60:63], v[144:147], v[112:115]\n"
      "ds_read_b64_tr_b16 v[40:41], %[wa2] offset:49152\n"
      "v_mfma_f32_16x16x32_bf16 v[116:119], v[60:63], v[148:151], v[116:119]\n"
      "ds_read_b64_tr_b16 v[42:43], %[wa2] offset:49664\n"
      "v_mfma_f32_16x16x32_bf16 v[120:123], v[60:63], v[152:155], v[120:123]\n"
      "ds_read_b64_tr_b16 v[44:45], %[wa3] offset:49152\n"
      "v_mfma_f32_16x16x32_bf16 v[124:127], v[60:63], v[156:159], v[124:127]\n"
      "ds_read_b64_tr_b16 v[46:47], %[wa3] offset:49664\n"
      "s_waitcnt lgkmcnt(9)\n"
      "v_mfma_f32_16x16x32_bf16 v[64:67], v[32:35], v[128:131], v[64:67]\n"
      "ds_read_b64_tr_b16 v[48:49], %[wa0] offset:51200\n"
      "s_waitcnt lgkmcnt(9)\n"
      "v_mfma_f32_16x16x32_bf16 v[68:71], v[32:35], v[132:135], v[68:71]\n"
      "ds_read_b64_tr_b16 v[50:51], %[wa0] offset:51712\n"
      "s_waitcnt lgkmcnt(9)\n"
      "v_mfma_f32_16x16x32_bf16 v[72:75], v[32:35], v[136:139], v[72:75]\n"
      "ds_read_b128 v[144:147], %[xa1] offset:0\n"
      "s_waitcnt lgkmcnt(9)\n"
      "v_mfma_f32_16x16x32_bf16 v[76:79], v[32:35], v[140:143], v[76:79]\n"
      "ds_read_b128 v[148:151], %[xa1] offset:2048\n"
      "s_waitcnt lgkmcnt(8)\n"
      "v_mfma_f32_16x16x32_bf16 v[80:83], v[36:39], v[128:131], v[80:83]\n"
      "ds_read_b128 v[152:155], %[xa1] offset:4096\n"
      "v_mfma_f32_16x16x32_bf16 v[84:87], v[36:39], v[132:135], v[84:87]\n"
      "ds_read_b128 v[156:159], %[xa1] offset:6144\n"
      "v_add_u32 %[xa1], %[d2], %[xa1]\n"
      "v_mfma_f32_16x16x32_bf16 v[88:91], v[36:39], v[136:139], v[88:91]\n"
      "ds_read_b64_tr_b16 v[52:53], %[wa1] offset:51200\n"
      "v_mfma_f32_16x16x32_bf16 v[92:95], v[36:39], v[140:143], v[92:95]\n"
      "ds_read_b64_tr_b16 v[54:55], %[wa1] offset:51712\n"
      "s_waitcnt lgkmcnt(10)\n"
      "v_mfma_f32_16x16x32_bf16 v[96:99], v[40:43], v[128:131], v[96:99]\n"
      "ds_read_b64_tr_b16 v[56:57], %[wa2] offset:51200\n"
      "v_mfma_f32_16x16x32_bf16 v[100:103], v[40:43], v[132:135], v[100:103]\n"
      "ds_read_b64_tr_b16 v[58:59], %[wa2] offset:51712\n"
      "v_mfma_f32_16x16x32_bf16 v[104:107], v[40:43], v[136:139], v[104:107]\n"
      "ds_read_b64_tr_b16 v[60:61], %[wa3] offset:51200\n"
      "v_mfma_f32_16x16x32_bf16 v[108:111], v[40:43], v[140:143], v[108:111]\n"
      "ds_read_b64_tr_b16 v[62:63], %[wa3] offset:51712\n"
      "s_waitcnt lgkmcnt(12)\n"
      "v_mfma_f32_16x16x32_bf16 v[112:115], v[44:47], v[128:131], v[112:115]\n"
      "v_mfma_f32_16x16x32_bf16 v[116:119], v[44:47], v[132:135], v[116:119]\n"
      "v_mfma_f32_16x16x32_bf16 v[120:123], v[44:47], v[136:139], v[120:123]\n"
      "v_mfma_f32_16x16x32_bf16 v[124:127], v[44:47], v[140:143], v[124:127]\n"
      "s_waitcnt lgkmcnt(9)\n"
      "v_mfma_f32_16x16x32_bf16 v[64:67], v[48:51], v[144:147], v[64:67]\n"
      "s_waitcnt lgkmcnt(8)\n"
      "v_mfma_f32_16x16x32_bf16 v[68:71], v[48:51], v[148:151], v[68:71]\n"
      "s_waitcnt lgkmcnt(7)\n"
      "v_mfma_f32_16x16x32_bf16 v[72:75], v[48:51], v[152:155], v[72:75]\n"
      "s_waitcnt lgkmcnt(6)\n"
      "v_mfma_f32_16x16x32_bf16 v[76:79], v[48:51], v[156:159], v[76:79]\n"
      "s_waitcnt lgkmcnt(4)\n"
      "s_waitcnt lgkmcnt(0)\n"
      "s_barrier\n"
      "v_mfma_f32_16x16x32_bf16 v[80:83], v[52:55], v[144:147], v[80:83]\n"
      "ds_read_b64_tr_b16 v[32:33], %[wa0] offset:0\n"
      "v_mfma_f32_16x16x32_bf16 v[84:87], v[52:55], v[148:151], v[84:87]\n"
      "ds_read_b64_tr_b16 v[34:35], %[wa0] offset:512\n"
      "v_mfma_f32_16x16x32_bf16 v[88:91], v[52:55], v[152:155], v[88:91]\n"
      "ds_read_b128 v[128:131], %[xa0] offset:0\n"
      "v_mfma_f32_16x16x32_bf16 v[92:95], v[52:55], v[156:159], v[92:95]\n"
      "ds_read_b128 v[132:135], %[xa0] offset:2048\n"
      "v_mfma_f32_16x16x32_bf16 v[96:99], v[56:59], v[144:147], v[96:99]\n"
      "ds_read_b128 v[136:139], %[xa0] offset:4096\n"
      "v_mfma_f32_16x16x32_bf16 v[100:103], v[56:59], v[148:151], v[100:103]\n"
      "ds_read_b128 v[140:143], %[xa0] offset:6144\n"
      "v_add_u32 %[xa0], %[d0], %[xa0]\n"
      "v_mfma_f32_16x16x32_bf16 v[104:107], v[56:59], v[152:155], v[104:107]\n"
      "ds_read_b64_tr_b16 v[36:37], %[wa1] offset:0\n"
      "v_mfma_f32_16x16x32_bf16 v[108:111], v[56:59], v[156:159], v[108:111]\n"
      "ds_read_b64_tr_b16 v[38:39], %[wa1] offset:512\n"
      "v_mfma_f32_16x16x32_bf16 v[112:115], v[60:63], v[144:147], v[112:115]\n"
      "ds_read_b64_tr_b16 v[40:41], %[wa2] offset:0\n"
      "v_mfma_f32_16x16x32_bf16 v[116:119], v[60:63], v[148:151], v[116:119]\n"
      "ds_read_b64_tr_b16 v[42:43], %[wa2] offset:512\n"
      "v_mfma_f32_16x16x32_bf16 v[120:123], v[60:63], v[152:155], v[120:123]\n"
      "ds_read_b64_tr_b16 v[44:45], %[wa3] offset:0\n"
      "v_mfma_f32_16x16x32_bf16 v[124:127], v[60:63], v[156:159], v[124:127]\n"
      "ds_read_b64_tr_b16 v[46:47], %[wa3] offset:512\n"
      "s_waitcnt lgkmcnt(9)\n"
      "v_mfma_f32_16x16x32_bf16 v[64:67], v[32:35], v[128:131], v[64:67]\n"
      "ds_read_b64_tr_b16 v[48:49], %[wa0] offset:2048\n"
      "s_waitcnt lgkmcnt(9)\n"
      "v_mfma_f32_16x16x32_bf16 v[68:71], v[32:35], v[132:135], v[68:71]\n"
      "ds_read_b64_tr_b16 v[50:51], %[wa0] offset:2560\n"
      "s_waitcnt lgkmcnt(9)\n"
      "v_mfma_f32_16x16x32_bf16 v[72:75], v[32:35], v[136:139], v[72:75]\n"
      "ds_read_b128 v[144:147], %[xa1] offset:0\n"
      "s_waitcnt lgkmcnt(9)\n"
      "v_mfma_f32_16x16x32_bf16 v[76:79], v[32:35], v[140:143], v[76:79]\n"
      "ds_read_b128 v[148:151], %[xa1] offset:2048\n"
      "s_waitcnt lgkmcnt(8)\n"
      "v_mfma_f32_16x16x32_bf16 v[80:83], v[36:39], v[128:131], v[80:83]\n"
      "ds_read_b128 v[152:155], %[xa1] offset:4096\n"
      "v_mfma_f32_16x16x32_bf16 v[84:87], v[36:39], v[132:135], v[84:87]\n"
      "ds_read_b128 v[156:159], %[xa1] offset:6144\n"
      "v_add_u32 %[xa1], %[d0], %[xa1]\n"
      "v_mfma_f32_16x16x32_bf16 v[88:91], v[36:39], v[136:139], v[88:91]\n"
      "ds_read_b64_tr_b16 v[52:53], %[wa1] offset:2048\n"
      "v_mfma_f32_16x16x32_bf16 v[92:95], v[36:39], v[140:143], v[92:95]\n"
      "ds_read_b64_tr_b16 v[54:55], %[wa1] offset:2560\n"
      "s_waitcnt lgkmcnt(10)\n"
      "v_mfma_f32_16x16x32_bf16 v[96:99], v[40:43], v[128:131], v[96:99]\n"
      "ds_read_b64_tr_b16 v[56:57], %[wa2] offset:2048\n"
      "v_mfma_f32_16x16x32_bf16 v[100:103], v[40:43], v[132:135], v[100:103]\n"
      "ds_read_b64_tr_b16 v[58:59], %[wa2] offset:2560\n"
      "v_mfma_f32_16x16x32_bf16 v[104:107], v[40:43], v[136:139], v[104:107]\n"
      "ds_read_b64_tr_b16 v[60:61], %[wa3] offset:2048\n"
      "v_mfma_f32_16x16x32_bf16 v[108:111], v[40:43], v[140:143], v[108:111]\n"
      "ds_read_b64_tr_b16 v[62:63], %[wa3] offset:2560\n"
      "s_waitcnt lgkmcnt(12)\n"
      "v_mfma_f32_16x16x32_bf16 v[112:115], v[44:47], v[128:131], v[112:115]\n"
      "v_mfma_f32_16x16x32_bf16 v[116:119], v[44:47], v[132:135], v[116:119]\n"
      "v_mfma_f32_16x16x32_bf16 v[120:123], v[44:47], v[136:139], v[120:123]\n"
      "v_mfma_f32_16x16x32_bf16 v[124:127], v[44:47], v[140:143], v[124:127]\n"
      "s_waitcnt lgkmcnt(9)\n"
      "v_mfma_f32_16x16x32_bf16 v[64:67], v[48:51], v[144:147], v[64:67]\n"
      "s_waitcnt lgkmcnt(8)\n"
      "v_mfma_f32_16x16x32_bf16 v[68:71], v[48:51], v[148:151], v[68:71]\n"
      "s_waitcnt lgkmcnt(7)\n"
      "v_mfma_f32_16x16x32_bf16 v[72:75], v[48:51], v[152:155], v[72:75]\n"
      "s_waitcnt lgkmcnt(6)\n"
      "v_mfma_f32_16x16x32_bf16 v[76:79], v[48:51], v[156:159], v[76:79]\n"
      "s_waitcnt lgkmcnt(4)\n"
      "s_waitcnt lgkmcnt(0)\n"
      "s_barrier\n"
      "v_mfma_f32_16x16x32_bf16 v[80:83], v[52:55], v[144:147], v[80:83]\n"
      "ds_read_b64_tr_b16 v[32:33], %[wa0] offset:24576\n"
      "v_mfma_f32_16x16x32_bf16 v[84:87], v[52:55], v[148:151], v[84:87]\n"
      "ds_read_b64_tr_b16 v[34:35], %[wa0] offset:25088\n"
      "v_mfma_f32_16x16x32_bf16 v[88:91], v[52:55], v[152:155], v[88:91]\n"
      "ds_read_b128 v[128:131], %[xa0] offset:0\n"
      "v_mfma_f32_16x16x32_bf16 v[92:95], v[52:55], v[156:159], v[92:95]\n"
      "ds_read_b128 v[132:135], %[xa0] offset:2048\n"
      "v_mfma_f32_16x16x32_bf16 v[96:99], v[56:59], v[144:147], v[96:99]\n"
      "ds_read_b128 v[136:139], %[xa0] offset:4096\n"
      "v_mfma_f32_16x16x32_bf16 v[100:103], v[56:59], v[148:151], v[100:103]\n"
      "ds_read_b128 v[140:143], %[xa0] offset:6144\n"
      "v_add_u32 %[xa0], %[d1], %[xa0]\n"
      "v_mfma_f32_16x16x32_bf16 v[104:107], v[56:59], v[152:155], v[104:107]\n"
      "ds_read_b64_tr_b16 v[36:37], %[wa1] offset:24576\n"
      "v_mfma_f32_16x16x32_bf16 v[108:111], v[56:59], v[156:159], v[108:111]\n"
      "ds_read_b64_tr_b16 v[38:39], %[wa1] offset:25088\n"
      "v_mfma_f32_16x16x32_bf16 v[112:115], v[60:63], v[144:147], v[112:115]\n"
      "ds_read_b64_tr_b16 v[40:41], %[wa2] offset:24576\n"
      "v_mfma_f32_16x16x32_bf16 v[116:119], v[60:63], v[148:151], v[116:119]\n"
      "ds_read_b64_tr_b16 v[42:43], %[wa2] offset:25088\n"
      "v_mfma_f32_16x16x32_bf16 v[120:123], v[60:63], v[152:155], v[120:123]\n"
      "ds_read_b64_tr_b16 v[44:45], %[wa3] offset:24576\n"
      "v_mfma_f32_16x16x32_bf16 v[124:127], v[60:63], v[156:159], v[124:127]\n"
      "ds_read_b64_tr_b16 v[46:47], %[wa3] offset:25088\n"
      "s_waitcnt lgkmcnt(9)\n"
      "v_mfma_f32_16x16x32_bf16 v[64:67], v[32:35], v[128:131], v[64:67]\n"
      "ds_read_b64_tr_b16 v[48:49], %[wa0] offset:26624\n"
      "s_waitcnt lgkmcnt(9)\n"
      "v_mfma_f32_16x16x32_bf16 v[68:71], v[32:35], v[132:135], v[68:71]\n"
      "ds_read_b64_tr_b16 v[50:51], %[wa0] offset:27136\n"
      "s_waitcnt lgkmcnt(9)\n"
      "v_mfma_f32_16x16x32_bf16 v[72:75], v[32:35], v[136:139], v[72:75]\n"
      "ds_read_b128 v[144:147], %[xa1] offset:0\n"
      "s_waitcnt lgkmcnt(9)\n"
      "v_mfma_f32_16x16x32_bf16 v[76:79], v[32:35], v[140:143], v[76:79]\n"
      "ds_read_b128 v[148:151], %[xa1] offset:2048\n"
      "s_waitcnt lgkmcnt(8)\n"
      "v_mfma_f32_16x16x32_bf16 v[80:83], v[36:39], v[128:131], v[80:83]\n"
      "ds_read_b128 v[152:155], %[xa1] offset:4096\n"
      "v_mfma_f32_16x16x32_bf16 v[84:87], v[36:39], v[132:135], v[84:87]\n"
      "ds_read_b128 v[156:159], %[xa1] offset:6144\n"
      "v_add_u32 %[xa1], %[d1], %[xa1]\n"
      "v_mfma_f32_16x16x32_bf16 v[88:91], v[36:39], v[136:139], v[88:91]\n"
      "ds_read_b64_tr_b16 v[52:53], %[wa1] offset:26624\n"
      "v_mfma_f32_16x16x32_bf16 v[92:95], v[36:39], v[140:143], v[92:95]\n"
      "ds_read_b64_tr_b16 v[54:55], %[wa1] offset:27136\n"
      "s_waitcnt lgkmcnt(10)\n"
      "v_mfma_f32_16x16x32_bf16 v[96:99], v[40:43], v[128:131], v[96:99]\n"
      "ds_read_b64_tr_b16 v[56:57], %[wa2] offset:26624\n"
      "v_mfma_f32_16x16x32_bf16 v[100:103], v[40:43], v[132:135], v[100:103]\n"
      "ds_read_b64_tr_b16 v[58:59], %[wa2] offset:27136\n"
      "v_mfma_f32_16x16x32_bf16 v[104:107], v[40:43], v[136:139], v[104:107]\n"
      "ds_read_b64_tr_b16 v[60:61], %[wa3] offset:26624\n"
      "v_mfma_f32_16x16x32_bf16 v[108:111], v[40:43], v[140:143], v[108:111]\n"
      "ds_read_b64_tr_b16 v[62:63], %[wa3] offset:27136\n"
      "s_waitcnt lgkmcnt(12)\n"
      "v_mfma_f32_16x16x32_bf16 v[112:115], v[44:47], v[128:131], v[112:115]\n"
      "v_mfma_f32_16x16x32_bf16 v[116:119], v[44:47], v[132:135], v[116:119]\n"
      "v_mfma_f32_16x16x32_bf16 v[120:123], v[44:47], v[136:139], v[120:123]\n"
      "v_mfma_f32_16x16x32_bf16 v[124:127], v[44:47], v[140:143], v[124:127]\n"
      "s_waitcnt lgkmcnt(9)\n"
      "v_mfma_f32_16x16x32_bf16 v[64:67], v[48:51], v[144:147], v[64:67]\n"
      "s_waitcnt lgkmcnt(8)\n"
      "v_mfma_f32_16x16x32_bf16 v[68:71], v[48:51], v[148:151], v[68:71]\n"
      "s_waitcnt lgkmcnt(7)\n"
      "v_mfma_f32_16x16x32_bf16 v[72:75], v[48:51], v[152:155], v[72:75]\n"
      "s_waitcnt lgkmcnt(6)\n"
      "v_mfma_f32_16x16x32_bf16 v[76:79], v[48:51], v[156:159], v[76:79]\n"
      "s_waitcnt lgkmcnt(4)\n"
      "s_waitcnt lgkmcnt(0)\n"
      "s_barrier\n"
      "v_mfma_f32_16x16x32_bf16 v[80:83], v[52:55], v[144:147], v[80:83]\n"
      "ds_read_b64_tr_b16 v[32:33], %[wa0] offset:49152\n"
      "v_mfma_f32_16x16x32_bf16 v[84:87], v[52:55], v[148:151], v[84:87]\n"
      "ds_read_b64_tr_b16 v[34:35], %[wa0] offset:49664\n"
      "v_mfma_f32_16x16x32_bf16 v[88:91], v[52:55], v[152:155], v[88:91]\n"
      "ds_read_b128 v[128:131], %[xa0] offset:0\n"
      "v_mfma_f32_16x16x32_bf16 v[92:95], v[52:55], v[156:159], v[92:95]\n"
      "ds_read_b128 v[132:135], %[xa0] offset:2048\n"
      "v_mfma_f32_16x16x32_bf16 v[96:99], v[56:59], v[144:147], v[96:99]\n"
      "ds_read_b128 v[136:139], %[xa0] offset:4096\n"
      "v_mfma_f32_16x16x32_bf16 v[100:103], v[56:59], v[148:151], v[100:103]\n"
      "ds_read_b128 v[140:143], %[xa0] offset:6144\n"
      "v_add_u32 %[xa0], %[d2], %[xa0]\n"
      "v_mfma_f32_16x16x32_bf16 v[104:107], v[56:59], v[152:155], v[104:107]\n"
      "ds_read_b64_tr_b16 v[36:37], %[wa1] offset:49152\n"
      "v_mfma_f32_16x16x32_bf16 v[108:111], v[56:59], v[156:159], v[108:111]\n"
      "ds_read_b64_tr_b16 v[38:39], %[wa1] offset:49664\n"
      "v_mfma_f32_16x16x32_bf16 v[112:115], v[60:63], v[144:147], v[112:115]\n"
      "ds_read_b64_tr_b16 v[40:41], %[wa2] offset:49152\n"
      "v_mfma_f32_16x16x32_bf16 v[116:119], v[60:63], v[148:151], v[116:119]\n"
      "ds_read_b64_tr_b16 v[42:43], %[wa2] offset:49664\n"
      "v_mfma_f32_16x16x32_bf16 v[120:123], v[60:63], v[152:155], v[120:123]\n"
      "ds_read_b64_tr_b16 v[44:45], %[wa3] offset:49152\n"
      "v_mfma_f32_16x16x32_bf16 v[124:127], v[60:63], v[156:159], v[124:127]\n"
      "ds_read_b64_tr_b16 v[46:47], %[wa3] offset:49664\n"
      "s_waitcnt lgkmcnt(9)\n"
      "v_mfma_f32_16x16x32_bf16 v[64:67], v[32:35], v[128:131], v[64:67]\n"
      "ds_read_b64_tr_b16 v[48:49], %[wa0] offset:51200\n"
      "s_waitcnt lgkmcnt(9)\n"
      "v_mfma_f32_16x16x32_bf16 v[68:71], v[32:35], v[132:135], v[68:71]\n"
      "ds_read_b64_tr_b16 v[50:51], %[wa0] offset:51712\n"
      "s_waitcnt lgkmcnt(9)\n"
      "v_mfma_f32_16x16x32_bf16 v[72:75], v[32:35], v[136:139], v[72:75]\n"
      "ds_read_b128 v[144:147], %[xa1] offset:0\n"
      "s_waitcnt lgkmcnt(9)\n"
      "v_mfma_f32_16x16x32_bf16 v[76:79], v[32:35], v[140:143], v[76:79]\n"
      "ds_read_b128 v[148:151], %[xa1] offset:2048\n"
      "s_waitcnt lgkmcnt(8)\n"
      "v_mfma_f32_16x16x32_bf16 v[80:83], v[36:39], v[128:131], v[80:83]\n"
      "ds_read_b128 v[152:155], %[xa1] offset:4096\n"
      "v_mfma_f32_16x16x32_bf16 v[84:87], v[36:39], v[132:135], v[84:87]\n"
      "ds_read_b128 v[156:159], %[xa1] offset:6144\n"
      "v_add_u32 %[xa1], %[d2], %[xa1]\n"
      "v_mfma_f32_16x16x32_bf16 v[88:91], v[36:39], v[136:139], v[88:91]\n"
      "ds_read_b64_tr_b16 v[52:53], %[wa1] offset:51200\n"
      "v_mfma_f32_16x16x32_bf16 v[92:95], v[36:39], v[140:143], v[92:95]\n"
      "ds_read_b64_tr_b16 v[54:55], %[wa1] offset:51712\n"
      "s_waitcnt lgkmcnt(10)\n"
      "v_mfma_f32_16x16x32_bf16 v[96:99], v[40:43], v[128:131], v[96:99]\n"
      "ds_read_b64_tr_b16 v[56:57], %[wa2] offset:51200\n"
      "v_mfma_f32_16x16x32_bf16 v[100:103], v[40:43], v[132:135], v[100:103]\n"
      "ds_read_b64_tr_b16 v[58:59], %[wa2] offset:51712\n"
      "v_mfma_f32_16x16x32_bf16 v[104:107], v[40:43], v[136:139], v[104:107]\n"
      "ds_read_b64_tr_b16 v[60:61], %[wa3] offset:51200\n"
      "v_mfma_f32_16x16x32_bf16 v[108:111], v[40:43], v[140:143], v[108:111]\n"
      "ds_read_b64_tr_b16 v[62:63], %[wa3] offset:51712\n"
      "s_waitcnt lgkmcnt(12)\n"
      "v_mfma_f32_16x16x32_bf16 v[112:115], v[44:47], v[128:131], v[112:115]\n"
      "v_mfma_f32_16x16x32_bf16 v[116:119], v[44:47], v[132:135], v[116:119]\n"
      "v_mfma_f32_16x16x32_bf16 v[120:123], v[44:47], v[136:139], v[120:123]\n"
      "v_mfma_f32_16x16x32_bf16 v[124:127], v[44:47], v[140:143], v[124:127]\n"
      "s_waitcnt lgkmcnt(9)\n"
      "v_mfma_f32_16x16x32_bf16 v[64:67], v[48:51], v[144:147], v[64:67]\n"
      "s_waitcnt lgkmcnt(8)\n"
      "v_mfma_f32_16x16x32_bf16 v[68:71], v[48:51], v[148:151], v[68:71]\n"
      "s_waitcnt lgkmcnt(7)\n"
      "v_mfma_f32_16x16x32_bf16 v[72:75], v[48:51], v[152:155], v[72:75]\n"
      "s_waitcnt lgkmcnt(6)\n"
      "v_mfma_f32_16x16x32_bf16 v[76:79], v[48:51], v[156:159], v[76:79]\n"
      "s_waitcnt lgkmcnt(4)\n"
      "s_waitcnt lgkmcnt(0)\n"
      "s_barrier\n"
      "v_mfma_f32_16x16x32_bf16 v[80:83], v[52:55], v[144:147], v[80:83]\n"
      "ds_read_b64_tr_b16 v[32:33], %[wa0] offset:0\n"
      "v_mfma_f32_16x16x32_bf16 v[84:87], v[52:55], v[148:151], v[84:87]\n"
      "ds_read_b64_tr_b16 v[34:35], %[wa0] offset:512\n"
      "v_mfma_f32_16x16x32_bf16 v[88:91], v[52:55], v[152:155], v[88:91]\n"
      "ds_read_b128 v[128:131], %[xa0] offset:0\n"
      "v_mfma_f32_16x16x32_bf16 v[92:95], v[52:55], v[156:159], v[92:95]\n"
      "ds_read_b128 v[132:135], %[xa0] offset:2048\n"
      "v_mfma_f32_16x16x32_bf16 v[96:99], v[56:59], v[144:147], v[96:99]\n"
      "ds_read_b128 v[136:139], %[xa0] offset:4096\n"
      "v_mfma_f32_16x16x32_bf16 v[100:103], v[56:59], v[148:151], v[100:103]\n"
      "ds_read_b128 v[140:143], %[xa0] offset:6144\n"
      "v_add_u32 %[xa0], %[d0], %[xa0]\n"
      "v_mfma_f32_16x16x32_bf16 v[104:107], v[56:59], v[152:155], v[104:107]\n"
      "ds_read_b64_tr_b16 v[36:37], %[wa1] offset:0\n"
      "v_mfma_f32_16x16x32_bf16 v[108:111], v[56:59], v[156:159], v[108:111]\n"
      "ds_read_b64_tr_b16 v[38:39], %[wa1] offset:512\n"
      "v_mfma_f32_16x16x32_bf16 v[112:115], v[60:63], v[144:147], v[112:115]\n"
      "ds_read_b64_tr_b16 v[40:41], %[wa2] offset:0\n"
      "v_mfma_f32_16x16x32_bf16 v[116:119], v[60:63], v[148:151], v[116:119]\n"
      "ds_read_b64_tr_b16 v[42:43], %[wa2] offset:512\n"
      "v_mfma_f32_16x16x32_bf16 v[120:123], v[60:63], v[152:155], v[120:123]\n"
      "ds_read_b64_tr_b16 v[44:45], %[wa3] offset:0\n"
      "v_mfma_f32_16x16x32_bf16 v[124:127], v[60:63], v[156:159], v[124:127]\n"
      "ds_read_b64_tr_b16 v[46:47], %[wa3] offset:512\n"
      "s_waitcnt lgkmcnt(9)\n"
      "v_mfma_f32_16x16x32_bf16 v[64:67], v[32:35], v[128:131], v[64:67]\n"
      "ds_read_b64_tr_b16 v[48:49], %[wa0] offset:2048\n"
      "s_waitcnt lgkmcnt(9)\n"
      "v_mfma_f32_16x16x32_bf16 v[68:71], v[32:35], v[132:135], v[68:71]\n"
      "ds_read_b64_tr_b16 v[50:51], %[wa0] offset:2560\n"
      "s_waitcnt lgkmcnt(9)\n"
      "v_mfma_f32_16x16x32_bf16 v[72:75], v[32:35], v[136:139], v[72:75]\n"
      "ds_read_b128 v[144:147], %[xa1] offset:0\n"
      "s_waitcnt lgkmcnt(9)\n"
      "v_mfma_f32_16x16x32_bf16 v[76:79], v[32:35], v[140:143], v[76:79]\n"
      "ds_read_b128 v[148:151], %[xa1] offset:2048\n"
      "s_waitcnt lgkmcnt(8)\n"
      "v_mfma_f32_16x16x32_bf16 v[80:83], v[36:39], v[128:131], v[80:83]\n"
      "ds_read_b128 v[152:155], %[xa1] offset:4096\n"
      "v_mfma_f32_16x16x32_bf16 v[84:87], v[36:39], v[132:135], v[84:87]\n"
      "ds_read_b128 v[156:159], %[xa1] offset:6144\n"
      "v_add_u32 %[xa1], %[d0], %[xa1]\n"
      "v_mfma_f32_16x16x32_bf16 v[88:91], v[36:39], v[136:139], v[88:91]\n"
      "ds_read_b64_tr_b16 v[52:53], %[wa1] offset:2048\n"
      "v_mfma_f32_16x16x32_bf16 v[92:95], v[36:39], v[140:143], v[92:95]\n"
      "ds_read_b64_tr_b16 v[54:55], %[wa1] offset:2560\n"
      "s_waitcnt lgkmcnt(10)\n"
      "v_mfma_f32_16x16x32_bf16 v[96:99], v[40:43], v[128:131], v[96:99]\n"
      "ds_read_b64_tr_b16 v[56:57], %[wa2] offset:2048\n"
      "v_mfma_f32_16x16x32_bf16 v[100:103], v[40:43], v[132:135], v[100:103]\n"
      "ds_read_b64_tr_b16 v[58:59], %[wa2] offset:2560\n"
      "v_mfma_f32_16x16x32_bf16 v[104:107], v[40:43], v[136:139], v[104:107]\n"
      "ds_read_b64_tr_b16 v[60:61], %[wa3] offset:2048\n"
      "v_mfma_f32_16x16x32_bf16 v[108:111], v[40:43], v[140:143], v[108:111]\n"
      "ds_read_b64_tr_b16 v[62:63], %[wa3] offset:2560\n"
      "s_waitcnt lgkmcnt(12)\n"
      "v_mfma_f32_16x16x32_bf16 v[112:115], v[44:47], v[128:131], v[112:115]\n"
      "v_mfma_f32_16x16x32_bf16 v[116:119], v[44:47], v[132:135], v[116:119]\n"
      "v_mfma_f32_16x16x32_bf16 v[120:123], v[44:47], v[136:139], v[120:123]\n"
      "v_mfma_f32_16x16x32_bf16 v[124:127], v[44:47], v[140:143], v[124:127]\n"
      "s_waitcnt lgkmcnt(9)\n"
      "v_mfma_f32_16x16x32_bf16 v[64:67], v[48:51], v[144:147], v[64:67]\n"
      "s_waitcnt lgkmcnt(8)\n"
      "v_mfma_f32_16x16x32_bf16 v[68:71], v[48:51], v[148:151], v[68:71]\n"
      "s_waitcnt lgkmcnt(7)\n"
      "v_mfma_f32_16x16x32_bf16 v[72:75], v[48:51], v[152:155], v[72:75]\n"
      "s_waitcnt lgkmcnt(6)\n"
      "v_mfma_f32_16x16x32_bf16 v[76:79], v[48:51], v[156:159], v[76:79]\n"
      "s_waitcnt lgkmcnt(4)\n"
      "s_waitcnt lgkmcnt(0)\n"
      "s_barrier\n"
      "v_mfma_f32_16x16x32_bf16 v[80:83], v[52:55], v[144:147], v[80:83]\n"
      "v_mfma_f32_16x16x32_bf16 v[84:87], v[52:55], v[148:151], v[84:87]\n"
      "v_mfma_f32_16x16x32_bf16 v[88:91], v[52:55], v[152:155], v[88:91]\n"
      "v_mfma_f32_16x16x32_bf16 v[92:95], v[52:55], v[156:159], v[92:95]\n"
      "v_mfma_f32_16x16x32_bf16 v[96:99], v[56:59], v[144:147], v[96:99]\n"
      "v_mfma_f32_16x16x32_bf16 v[100:103], v[56:59], v[148:151], v[100:103]\n"
      "v_mfma_f32_16x16x32_bf16 v[104:107], v[56:59], v[152:155], v[104:107]\n"
      "v_mfma_f32_16x16x32_bf16 v[108:111], v[56:59], v[156:159], v[108:111]\n"
      "v_mfma_f32_16x16x32_bf16 v[112:115], v[60:63], v[144:147], v[112:115]\n"
      "v_mfma_f32_16x16x32_bf16 v[116:119], v[60:63], v[148:151], v[116:119]\n"
      "v_mfma_f32_16x16x32_bf16 v[120:123], v[60:63], v[152:155], v[120:123]\n"
      "v_mfma_f32_16x16x32_bf16 v[124:127], v[60:63], v[156:159], v[124:127]\n"
      "s_nop 7\n"
        : "=&{v[64:79]}"(acc[0]), "=&{v[80:95]}"(acc[1]), "=&{v[96:111]}"(acc[2]), "=&{v[112:127]}"(acc[3]), [xa0] "+v"(xa0), [xa1] "+v"(xa1)
        : [wa0] "v"(wq[0]), [wa1] "v"(wq[1]), [wa2] "v"(wq[2]), [wa3] "v"(wq[3]), [d0] "s"(d0), [d1] "s"(d0), [d2] "s"(d2)
        : "v32", "v33", "v34", "v35", "v36", "v37", "v38", "v39", "v40", "v41", "v42", "v43", "v44", "v45", "v46", "v47", "v48", "v49", "v50", "v51", "v52", "v53", "v54", "v55", "v56", "v57", "v58", "v59", "v60", "v61", "v62", "v63", "v128", "v129", "v130", "v131", "v132", "v133", "v134", "v135", "v136", "v137", "v138", "v139", "v140", "v141", "v142", "v143", "v144", "v145", "v146", "v147", "v148", "v149", "v150", "v151", "v152", "v153", "v154", "v155", "v156", "v157", "v158", "v159", "memory");
      const float sc = isq ? QSCALE : 1.0f;
      const u32 t0 = isq ? WS_Q + (4 * rb + 2 * wm) * Q_TILE : WS_KV + (2 * rb + wm) * KV_PAIR + 1280;
#pragma unroll
      for (int n = 0; n < 4; ++n) {
        const u32 da = ea[n];
        const f32x16 a = acc[n] * sc;
#pragma unroll
        for (int p = 0; p < 2; ++p) {
          const u32x4 v = pairimg(a, p);
          if (n < 3) {
            st16(rs, v, da, t0 + p * 1280);
          } else {
            st8(rs, u32x2{v[0], v[1]}, da, t0 + p * 1280);
            st8(rs, u32x2{v[2], v[3]}, ea[4], t0 + p * 1280);
          }
        }
      }
    } else {
      asm volatile(
      "s_barrier\n"
      "ds_read_b64_tr_b16 v[32:33], %[wa0] offset:0\n"
      "ds_read_b64_tr_b16 v[34:35], %[wa0] offset:512\n"
      "ds_read_b128 v[128:131], %[xa0] offset:0\n"
      "ds_read_b128 v[132:135], %[xa0] offset:2048\n"
      "ds_read_b128 v[136:139], %[xa0] offset:4096\n"
      "ds_read_b128 v[140:143], %[xa0] offset:6144\n"
      "ds_read_b64_tr_b16 v[36:37], %[wa1] offset:0\n"
      "ds_read_b64_tr_b16 v[38:39], %[wa1] offset:512\n"
      "ds_read_b64_tr_b16 v[40:41], %[wa2] offset:0\n"
      "ds_read_b64_tr_b16 v[42:43], %[wa2] offset:512\n"
      "ds_read_b64_tr_b16 v[44:45], %[wa3] offset:0\n"
      "ds_read_b64_tr_b16 v[46:47], %[wa3] offset:512\n"
      "v_add_u32 %[xa0], %[d0], %[xa0]\n"
      "s_waitcnt lgkmcnt(9)\n"
      "v_mfma_f32_16x16x32_bf16 v[64:67], v[128:131], v[32:35], 0\n"
      "ds_read_b64_tr_b16 v[48:49], %[wa0] offset:2048\n"
      "s_waitcnt lgkmcnt(9)\n"
      "v_mfma_f32_16x16x32_bf16 v[68:71], v[132:135], v[32:35], 0\n"
      "ds_read_b64_tr_b16 v[50:51], %[wa0] offset:2560\n"
      "s_waitcnt lgkmcnt(9)\n"
      "v_mfma_f32_16x16x32_bf16 v[72:75], v[136:139], v[32:35], 0\n"
      "ds_read_b128 v[144:147], %[xa1] offset:0\n"
      "s_waitcnt lgkmcnt(9)\n"
      "v_mfma_f32_16x16x32_bf16 v[76:79], v[140:143], v[32:35], 0\n"
      "ds_read_b128 v[148:151], %[xa1] offset:2048\n"
      "s_waitcnt lgkmcnt(8)\n"
      "v_mfma_f32_16x16x32_bf16 v[80:83], v[128:131], v[36:39], 0\n"
      "ds_read_b128 v[152:155], %[xa1] offset:4096\n"
      "v_mfma_f32_16x16x32_bf16 v[84:87], v[132:135], v[36:39], 0\n"
      "ds_read_b128 v[156:159], %[xa1] offset:6144\n"
      "v_add_u32 %[xa1], %[d0], %[xa1]\n"
      "v_mfma_f32_16x16x32_bf16 v[88:91], v[136:139], v[36:39], 0\n"
      "ds_read_b64_tr_b16 v[52:53], %[wa1] offset:2048\n"
      "v_mfma_f32_16x16x32_bf16 v[92:95], v[140:143], v[36:39], 0\n"
      "ds_read_b64_tr_b16 v[54:55], %[wa1] offset:2560\n"
      "s_waitcnt lgkmcnt(10)\n"
      "v_mfma_f32_16x16x32_bf16 v[96:99], v[128:131], v[40:43], 0\n"
      "ds_read_b64_tr_b16 v[56:57], %[wa2] offset:2048\n"
      "v_mfma_f32_16x16x32_bf16 v[100:103], v[132:135], v[40:43], 0\n"
      "ds_read_b64_tr_b16 v[58:59], %[wa2] offset:2560\n"
      "v_mfma_f32_16x16x32_bf16 v[104:107], v[136:139], v[40:43], 0\n"
      "ds_read_b64_tr_b16 v[60:61], %[wa3] offset:2048\n"
      "v_mfma_f32_16x16x32_bf16 v[108:111], v[140:143], v[40:43], 0\n"
      "ds_read_b64_tr_b16 v[62:63], %[wa3] offset:2560\n"
      "s_waitcnt lgkmcnt(12)\n"
      "v_mfma_f32_16x16x32_bf16 v[112:115], v[128:131], v[44:47], 0\n"
      "v_mfma_f32_16x16x32_bf16 v[116:119], v[132:135], v[44:47], 0\n"
      "v_mfma_f32_16x16x32_bf16 v[120:123], v[136:139], v[44:47], 0\n"
      "v_mfma_f32_16x16x32_bf16 v[124:127], v[140:143], v[44:47], 0\n"
      "s_waitcnt lgkmcnt(9)\n"
      "v_mfma_f32_16x16x32_bf16 v[64:67], v[144:147], v[48:51], v[64:67]\n"
      "s_waitcnt lgkmcnt(8)\n"
      "v_mfma_f32_16x16x32_bf16 v[68:71], v[148:151], v[48:51], v[68:71]\n"
      "s_waitcnt lgkmcnt(7)\n"
      "v_mfma_f32_16x16x32_bf16 v[72:75], v[152:155], v[48:51], v[72:75]\n"
      "s_waitcnt lgkmcnt(6)\n"
      "v_mfma_f32_16x16x32_bf16 v[76:79], v[156:159], v[48:51], v[76:79]\n"
      "s_waitcnt lgkmcnt(4)\n"
      "s_waitcnt lgkmcnt(0)\n"
      "s_barrier\n"
      "v_mfma_f32_16x16x32_bf16 v[80:83], v[144:147], v[52:55], v[80:83]\n"
      "ds_read_b64_tr_b16 v[32:33], %[wa0] offset:24576\n"
      "v_mfma_f32_16x16x32_bf16 v[84:87], v[148:151], v[52:55], v[84:87]\n"
      "ds_read_b64_tr_b16 v[34:35], %[wa0] offset:25088\n"
      "v_mfma_f32_16x16x32_bf16 v[88:91], v[152:155], v[52:55], v[88:91]\n"
      "ds_read_b128 v[128:131], %[xa0] offset:0\n"
      "v_mfma_f32_16x16x32_bf16 v[92:95], v[156:159], v[52:55], v[92:95]\n"
      "ds_read_b128 v[132:135], %[xa0] offset:2048\n"
      "v_mfma_f32_16x16x32_bf16 v[96:99], v[144:147], v[56:59], v[96:99]\n"
      "ds_read_b128 v[136:139], %[xa0] offset:4096\n"
      "v_mfma_f32_16x16x32_bf16 v[100:103], v[148:151], v[56:59], v[100:103]\n"
      "ds_read_b128 v[140:143], %[xa0] offset:6144\n"
      "v_add_u32 %[xa0], %[d1], %[xa0]\n"
      "v_mfma_f32_16x16x32_bf16 v[104:107], v[152:155], v[56:59], v[104:107]\n"
      "ds_read_b64_tr_b16 v[36:37], %[wa1] offset:24576\n"
      "v_mfma_f32_16x16x32_bf16 v[108:111], v[156:159], v[56:59], v[108:111]\n"
      "ds_read_b64_tr_b16 v[38:39], %[wa1] offset:25088\n"
      "v_mfma_f32_16x16x32_bf16 v[112:115], v[144:147], v[60:63], v[112:115]\n"
      "ds_read_b64_tr_b16 v[40:41], %[wa2] offset:24576\n"
      "v_mfma_f32_16x16x32_bf16 v[116:119], v[148:151], v[60:63], v[116:119]\n"
      "ds_read_b64_tr_b16 v[42:43], %[wa2] offset:25088\n"
      "v_mfma_f32_16x16x32_bf16 v[120:123], v[152:155], v[60:63], v[120:123]\n"
      "ds_read_b64_tr_b16 v[44:45], %[wa3] offset:24576\n"
      "v_mfma_f32_16x16x32_bf16 v[124:127], v[156:159], v[60:63], v[124:127]\n"
      "ds_read_b64_tr_b16 v[46:47], %[wa3] offset:25088\n"
      "s_waitcnt lgkmcnt(9)\n"
      "v_mfma_f32_16x16x32_bf16 v[64:67], v[128:131], v[32:35], v[64:67]\n"
      "ds_read_b64_tr_b16 v[48:49], %[wa0] offset:26624\n"
      "s_waitcnt lgkmcnt(9)\n"
      "v_mfma_f32_16x16x32_bf16 v[68:71], v[132:135], v[32:35], v[68:71]\n"
      "ds_read_b64_tr_b16 v[50:51], %[wa0] offset:27136\n"
      "s_waitcnt lgkmcnt(9)\n"
      "v_mfma_f32_16x16x32_bf16 v[72:75], v[136:139], v[32:35], v[72:75]\n"
      "ds_read_b128 v[144:147], %[xa1] offset:0\n"
      "s_waitcnt lgkmcnt(9)\n"
      "v_mfma_f32_16x16x32_bf16 v[76:79], v[140:143], v[32:35], v[76:79]\n"
      "ds_read_b128 v[148:151], %[xa1] offset:2048\n"
      "s_waitcnt lgkmcnt(8)\n"
      "v_mfma_f32_16x16x32_bf16 v[80:83], v[128:131], v[36:39], v[80:83]\n"
      "ds_read_b128 v[152:155], %[xa1] offset:4096\n"
      "v_mfma_f32_16x16x32_bf16 v[84:87], v[132:135], v[36:39], v[84:87]\n"
      "ds_read_b128 v[156:159], %[xa1] offset:6144\n"
      "v_add_u32 %[xa1], %[d1], %[xa1]\n"
      "v_mfma_f32_16x16x32_bf16 v[88:91], v[136:139], v[36:39], v[88:91]\n"
      "ds_read_b64_tr_b16 v[52:53], %[wa1] offset:26624\n"
      "v_mfma_f32_16x16x32_bf16 v[92:95], v[140:143], v[36:39], v[92:95]\n"
      "ds_read_b64_tr_b16 v[54:55], %[wa1] offset:27136\n"
      "s_waitcnt lgkmcnt(10)\n"
      "v_mfma_f32_16x16x32_bf16 v[96:99], v[128:131], v[40:43], v[96:99]\n"
      "ds_read_b64_tr_b16 v[56:57], %[wa2] offset:26624\n"
      "v_mfma_f32_16x16x32_bf16 v[100:103], v[132:135], v[40:43], v[100:103]\n"
      "ds_read_b64_tr_b16 v[58:59], %[wa2] offset:27136\n"
      "v_mfma_f32_16x16x32_bf16 v[104:107], v[136:139], v[40:43], v[104:107]\n"
      "ds_read_b64_tr_b16 v[60:61], %[wa3] offset:26624\n"
      "v_mfma_f32_16x16x32_bf16 v[108:111], v[140:143], v[40:43], v[108:111]\n"
      "ds_read_b64_tr_b16 v[62:63], %[wa3] offset:27136\n"
      "s_waitcnt lgkmcnt(12)\n"
      "v_mfma_f32_16x16x32_bf16 v[112:115], v[128:131], v[44:47], v[112:115]\n"
      "v_mfma_f32_16x16x32_bf16 v[116:119], v[132:135], v[44:47], v[116:119]\n"
      "v_mfma_f32_16x16x32_bf16 v[120:123], v[136:139], v[44:47], v[120:123]\n"
      "v_mfma_f32_16x16x32_bf16 v[124:127], v[140:143], v[44:47], v[124:127]\n"
      "s_waitcnt lgkmcnt(9)\n"
      "v_mfma_f32_16x16x32_bf16 v[64:67], v[144:147], v[48:51], v[64:67]\n"
      "s_waitcnt lgkmcnt(8)\n"
      "v_mfma_f32_16x16x32_bf16 v[68:71], v[148:151], v[48:51], v[68:71]\n"
      "s_waitcnt lgkmcnt(7)\n"
      "v_mfma_f32_16x16x32_bf16 v[72:75], v[152:155], v[48:51], v[72:75]\n"
      "s_waitcnt lgkmcnt(6)\n"
      "v_mfma_f32_16x16x32_bf16 v[76:79], v[156:159], v[48:51], v[76:79]\n"
      "s_waitcnt lgkmcnt(4)\n"
      "s_waitcnt lgkmcnt(0)\n"
      "s_barrier\n"
      "v_mfma_f32_16x16x32_bf16 v[80:83], v[144:147], v[52:55], v[80:83]\n"
      "ds_read_b64_tr_b16 v[32:33], %[wa0] offset:49152\n"
      "v_mfma_f32_16x16x32_bf16 v[84:87], v[148:151], v[52:55], v[84:87]\n"
      "ds_read_b64_tr_b16 v[34:35], %[wa0] offset:49664\n"
      "v_mfma_f32_16x16x32_bf16 v[88:91], v[152:155], v[52:55], v[88:91]\n"
      "ds_read_b128 v[128:131], %[xa0] offset:0\n"
      "v_mfma_f32_16x16x32_bf16 v[92:95], v[156:159], v[52:55], v[92:95]\n"
      "ds_read_b128 v[132:135], %[xa0] offset:2048\n"
      "v_mfma_f32_16x16x32_bf16 v[96:99], v[144:147], v[56:59], v[96:99]\n"
      "ds_read_b128 v[136:139], %[xa0] offset:4096\n"
      "v_mfma_f32_16x16x32_bf16 v[100:103], v[148:151], v[56:59], v[100:103]\n"
      "ds_read_b128 v[140:143], %[xa0] offset:6144\n"
      "v_add_u32 %[xa0], %[d2], %[xa0]\n"
      "v_mfma_f32_16x16x32_bf16 v[104:107], v[152:155], v[56:59], v[104:107]\n"
      "ds_read_b64_tr_b16 v[36:37], %[wa1] offset:49152\n"
      "v_mfma_f32_16x16x32_bf16 v[108:111], v[156:159], v[56:59], v[108:111]\n"
      "ds_read_b64_tr_b16 v[38:39], %[wa1] offset:49664\n"
      "v_mfma_f32_16x16x32_bf16 v[112:115], v[144:147], v[60:63], v[112:115]\n"
      "ds_read_b64_tr_b16 v[40:41], %[wa2] offset:49152\n"
      "v_mfma_f32_16x16x32_bf16 v[116:119], v[148:151], v[60:63], v[116:119]\n"
      "ds_read_b64_tr_b16 v[42:43], %[wa2] offset:49664\n"
      "v_mfma_f32_16x16x32_bf16 v[120:123], v[152:155], v[60:63], v[120:123]\n"
      "ds_read_b64_tr_b16 v[44:45], %[wa3] offset:49152\n"
      "v_mfma_f32_16x16x32_bf16 v[124:127], v[156:159], v[60:63], v[124:127]\n"
      "ds_read_b64_tr_b16 v[46:47], %[wa3] offset:49664\n"
      "s_waitcnt lgkmcnt(9)\n"
      "v_mfma_f32_16x16x32_bf16 v[64:67], v[128:131], v[32:35], v[64:67]\n"
      "ds_read_b64_tr_b16 v[48:49], %[wa0] offset:51200\n"
      "s_waitcnt lgkmcnt(9)\n"
      "v_mfma_f32_16x16x32_bf16 v[68:71], v[132:135], v[32:35], v[68:71]\n"
      "ds_read_b64_tr_b16 v[50:51], %[wa0] offset:51712\n"
      "s_waitcnt lgkmcnt(9)\n"
      "v_mfma_f32_16x16x32_bf16 v[72:75], v[136:139], v[32:35], v[72:75]\n"
      "ds_read_b128 v[144:147], %[xa1] offset:0\n"
      "s_waitcnt lgkmcnt(9)\n"
      "v_mfma_f32_16x16x32_bf16 v[76:79], v[140:143], v[32:35], v[76:79]\n"
      "ds_read_b128 v[148:151], %[xa1] offset:2048\n"
      "s_waitcnt lgkmcnt(8)\n"
      "v_mfma_f32_16x16x32_bf16 v[80:83], v[128:131], v[36:39], v[80:83]\n"
      "ds_read_b128 v[152:155], %[xa1] offset:4096\n"
      "v_mfma_f32_16x16x32_bf16 v[84:87], v[132:135], v[36:39], v[84:87]\n"
      "ds_read_b128 v[156:159], %[xa1] offset:6144\n"
      "v_add_u32 %[xa1], %[d2], %[xa1]\n"
      "v_mfma_f32_16x16x32_bf16 v[88:91], v[136:139], v[36:39], v[88:91]\n"
      "ds_read_b64_tr_b16 v[52:53], %[wa1] offset:51200\n"
      "v_mfma_f32_16x16x32_bf16 v[92:95], v[140:143], v[36:39], v[92:95]\n"
      "ds_read_b64_tr_b16 v[54:55], %[wa1] offset:51712\n"
      "s_waitcnt lgkmcnt(10)\n"
      "v_mfma_f32_16x16x32_bf16 v[96:99], v[128:131], v[40:43], v[96:99]\n"
      "ds_read_b64_tr_b16 v[56:57], %[wa2] offset:51200\n"
      "v_mfma_f32_16x16x32_bf16 v[100:103], v[132:135], v[40:43], v[100:103]\n"
      "ds_read_b64_tr_b16 v[58:59], %[wa2] offset:51712\n"
      "v_mfma_f32_16x16x32_bf16 v[104:107], v[136:139], v[40:43], v[104:107]\n"
      "ds_read_b64_tr_b16 v[60:61], %[wa3] offset:51200\n"
      "v_mfma_f32_16x16x32_bf16 v[108:111], v[140:143], v[40:43], v[108:111]\n"
      "ds_read_b64_tr_b16 v[62:63], %[wa3] offset:51712\n"
      "s_waitcnt lgkmcnt(12)\n"
      "v_mfma_f32_16x16x32_bf16 v[112:115], v[128:131], v[44:47], v[112:115]\n"
      "v_mfma_f32_16x16x32_bf16 v[116:119], v[132:135], v[44:47], v[116:119]\n"
      "v_mfma_f32_16x16x32_bf16 v[120:123], v[136:139], v[44:47], v[120:123]\n"
      "v_mfma_f32_16x16x32_bf16 v[124:127], v[140:143], v[44:47], v[124:127]\n"
      "s_waitcnt lgkmcnt(9)\n"
      "v_mfma_f32_16x16x32_bf16 v[64:67], v[144:147], v[48:51], v[64:67]\n"
      "s_waitcnt lgkmcnt(8)\n"
      "v_mfma_f32_16x16x32_bf16 v[68:71], v[148:151], v[48:51], v[68:71]\n"
      "s_waitcnt lgkmcnt(7)\n"
      "v_mfma_f32_16x16x32_bf16 v[72:75], v[152:155], v[48:51], v[72:75]\n"
      "s_waitcnt lgkmcnt(6)\n"
      "v_mfma_f32_16x16x32_bf16 v[76:79], v[156:159], v[48:51], v[76:79]\n"
      "s_waitcnt lgkmcnt(4)\n"
      "s_waitcnt lgkmcnt(0)\n"
      "s_barrier\n"
      "v_mfma_f32_16x16x32_bf16 v[80:83], v[144:147], v[52:55], v[80:83]\n"
      "ds_read_b64_tr_b16 v[32:33], %[wa0] offset:0\n"
      "v_mfma_f32_16x16x32_bf16 v[84:87], v[148:151], v[52:55], v[84:87]\n"
      "ds_read_b64_tr_b16 v[34:35], %[wa0] offset:512\n"
      "v_mfma_f32_16x16x32_bf16 v[88:91], v[152:155], v[52:55], v[88:91]\n"
      "ds_read_b128 v[128:131], %[xa0] offset:0\n"
      "v_mfma_f32_16x16x32_bf16 v[92:95], v[156:159], v[52:55], v[92:95]\n"
      "ds_read_b128 v[132:135], %[xa0] offset:2048\n"
      "v_mfma_f32_16x16x32_bf16 v[96:99], v[144:147], v[56:59], v[96:99]\n"
      "ds_read_b128 v[136:139], %[xa0] offset:4096\n"
      "v_mfma_f32_16x16x32_bf16 v[100:103], v[148:151], v[56:59], v[100:103]\n"
      "ds_read_b128 v[140:143], %[xa0] offset:6144\n"
      "v_add_u32 %[xa0], %[d0], %[xa0]\n"
      "v_mfma_f32_16x16x32_bf16 v[104:107], v[152:155], v[56:59], v[104:107]\n"
      "ds_read_b64_tr_b16 v[36:37], %[wa1] offset:0\n"
      "v_mfma_f32_16x16x32_bf16 v[108:111], v[156:159], v[56:59], v[108:111]\n"
      "ds_read_b64_tr_b16 v[38:39], %[wa1] offset:512\n"
      "v_mfma_f32_16x16x32_bf16 v[112:115], v[144:147], v[60:63], v[112:115]\n"
      "ds_read_b64_tr_b16 v[40:41], %[wa2] offset:0\n"
      "v_mfma_f32_16x16x32_bf16 v[116:119], v[148:151], v[60:63], v[116:119]\n"
      "ds_read_b64_tr_b16 v[42:43], %[wa2] offset:512\n"
      "v_mfma_f32_16x16x32_bf16 v[120:123], v[152:155], v[60:63], v[120:123]\n"
      "ds_read_b64_tr_b16 v[44:45], %[wa3] offset:0\n"
      "v_mfma_f32_16x16x32_bf16 v[124:127], v[156:159], v[60:63], v[124:127]\n"
      "ds_read_b64_tr_b16 v[46:47], %[wa3] offset:512\n"
      "s_waitcnt lgkmcnt(9)\n"
      "v_mfma_f32_16x16x32_bf16 v[64:67], v[128:131], v[32:35], v[64:67]\n"
      "ds_read_b64_tr_b16 v[48:49], %[wa0] offset:2048\n"
      "s_waitcnt lgkmcnt(9)\n"
      "v_mfma_f32_16x16x32_bf16 v[68:71], v[132:135], v[32:35], v[68:71]\n"
      "ds_read_b64_tr_b16 v[50:51], %[wa0] offset:2560\n"
      "s_waitcnt lgkmcnt(9)\n"
      "v_mfma_f32_16x16x32_bf16 v[72:75], v[136:139], v[32:35], v[72:75]\n"
      "ds_read_b128 v[144:147], %[xa1] offset:0\n"
      "s_waitcnt lgkmcnt(9)\n"
      "v_mfma_f32_16x16x32_bf16 v[76:79], v[140:143], v[32:35], v[76:79]\n"
      "ds_read_b128 v[148:151], %[xa1] offset:2048\n"
      "s_waitcnt lgkmcnt(8)\n"
      "v_mfma_f32_16x16x32_bf16 v[80:83], v[128:131], v[36:39], v[80:83]\n"
      "ds_read_b128 v[152:155], %[xa1] offset:4096\n"
      "v_mfma_f32_16x16x32_bf16 v[84:87], v[132:135], v[36:39], v[84:87]\n"
      "ds_read_b128 v[156:159], %[xa1] offset:6144\n"
      "v_add_u32 %[xa1], %[d0], %[xa1]\n"
      "v_mfma_f32_16x16x32_bf16 v[88:91], v[136:139], v[36:39], v[88:91]\n"
      "ds_read_b64_tr_b16 v[52:53], %[wa1] offset:2048\n"
      "v_mfma_f32_16x16x32_bf16 v[92:95], v[140:143], v[36:39], v[92:95]\n"
      "ds_read_b64_tr_b16 v[54:55], %[wa1] offset:2560\n"
      "s_waitcnt lgkmcnt(10)\n"
      "v_mfma_f32_16x16x32_bf16 v[96:99], v[128:131], v[40:43], v[96:99]\n"
      "ds_read_b64_tr_b16 v[56:57], %[wa2] offset:2048\n"
      "v_mfma_f32_16x16x32_bf16 v[100:103], v[132:135], v[40:43], v[100:103]\n"
      "ds_read_b64_tr_b16 v[58:59], %[wa2] offset:2560\n"
      "v_mfma_f32_16x16x32_bf16 v[104:107], v[136:139], v[40:43], v[104:107]\n"
      "ds_read_b64_tr_b16 v[60:61], %[wa3] offset:2048\n"
      "v_mfma_f32_16x16x32_bf16 v[108:111], v[140:143], v[40:43], v[108:111]\n"
      "ds_read_b64_tr_b16 v[62:63], %[wa3] offset:2560\n"
      "s_waitcnt lgkmcnt(12)\n"
      "v_mfma_f32_16x16x32_bf16 v[112:115], v[128:131], v[44:47], v[112:115]\n"
      "v_mfma_f32_16x16x32_bf16 v[116:119], v[132:135], v[44:47], v[116:119]\n"
      "v_mfma_f32_16x16x32_bf16 v[120:123], v[136:139], v[44:47], v[120:123]\n"
      "v_mfma_f32_16x16x32_bf16 v[124:127], v[140:143], v[44:47], v[124:127]\n"
      "s_waitcnt lgkmcnt(9)\n"
      "v_mfma_f32_16x16x32_bf16 v[64:67], v[144:147], v[48:51], v[64:67]\n"
      "s_waitcnt lgkmcnt(8)\n"
      "v_mfma_f32_16x16x32_bf16 v[68:71], v[148:151], v[48:51], v[68:71]\n"
      "s_waitcnt lgkmcnt(7)\n"
      "v_mfma_f32_16x16x32_bf16 v[72:75], v[152:155], v[48:51], v[72:75]\n"
      "s_waitcnt lgkmcnt(6)\n"
      "v_mfma_f32_16x16x32_bf16 v[76:79], v[156:159], v[48:51], v[76:79]\n"
      "s_waitcnt lgkmcnt(4)\n"
      "s_waitcnt lgkmcnt(0)\n"
      "s_barrier\n"
      "v_mfma_f32_16x16x32_bf16 v[80:83], v[144:147], v[52:55], v[80:83]\n"
      "ds_read_b64_tr_b16 v[32:33], %[wa0] offset:24576\n"
      "v_mfma_f32_16x16x32_bf16 v[84:87], v[148:151], v[52:55], v[84:87]\n"
      "ds_read_b64_tr_b16 v[34:35], %[wa0] offset:25088\n"
      "v_mfma_f32_16x16x32_bf16 v[88:91], v[152:155], v[52:55], v[88:91]\n"
      "ds_read_b128 v[128:131], %[xa0] offset:0\n"
      "v_mfma_f32_16x16x32_bf16 v[92:95], v[156:159], v[52:55], v[92:95]\n"
      "ds_read_b128 v[132:135], %[xa0] offset:2048\n"
      "v_mfma_f32_16x16x32_bf16 v[96:99], v[144:147], v[56:59], v[96:99]\n"
      "ds_read_b128 v[136:139], %[xa0] offset:4096\n"
      "v_mfma_f32_16x16x32_bf16 v[100:103], v[148:151], v[56:59], v[100:103]\n"
      "ds_read_b128 v[140:143], %[xa0] offset:6144\n"
      "v_add_u32 %[xa0], %[d1], %[xa0]\n"
      "v_mfma_f32_16x16x32_bf16 v[104:107], v[152:155], v[56:59], v[104:107]\n"
      "ds_read_b64_tr_b16 v[36:37], %[wa1] offset:24576\n"
      "v_mfma_f32_16x16x32_bf16 v[108:111], v[156:159], v[56:59], v[108:111]\n"
      "ds_read_b64_tr_b16 v[38:39], %[wa1] offset:25088\n"
      "v_mfma_f32_16x16x32_bf16 v[112:115], v[144:147], v[60:63], v[112:115]\n"
      "ds_read_b64_tr_b16 v[40:41], %[wa2] offset:24576\n"
      "v_mfma_f32_16x16x32_bf16 v[116:119], v[148:151], v[60:63], v[116:119]\n"
      "ds_read_b64_tr_b16 v[42:43], %[wa2] offset:25088\n"
      "v_mfma_f32_16x16x32_bf16 v[120:123], v[152:155], v[60:63], v[120:123]\n"
      "ds_read_b64_tr_b16 v[44:45], %[wa3] offset:24576\n"
      "v_mfma_f32_16x16x32_bf16 v[124:127], v[156:159], v[60:63], v[124:127]\n"
      "ds_read_b64_tr_b16 v[46:47], %[wa3] offset:25088\n"
      "s_waitcnt lgkmcnt(9)\n"
      "v_mfma_f32_16x16x32_bf16 v[64:67], v[128:131], v[32:35], v[64:67]\n"
      "ds_read_b64_tr_b16 v[48:49], %[wa0] offset:26624\n"
      "s_waitcnt lgkmcnt(9)\n"
      "v_mfma_f32_16x16x32_bf16 v[68:71], v[132:135], v[32:35], v[68:71]\n"
      "ds_read_b64_tr_b16 v[50:51], %[wa0] offset:27136\n"
      "s_waitcnt lgkmcnt(9)\n"
      "v_mfma_f32_16x16x32_bf16 v[72:75], v[136:139], v[32:35], v[72:75]\n"
      "ds_read_b128 v[144:147], %[xa1] offset:0\n"
      "s_waitcnt lgkmcnt(9)\n"
      "v_mfma_f32_16x16x32_bf16 v[76:79], v[140:143], v[32:35], v[76:79]\n"
      "ds_read_b128 v[148:151], %[xa1] offset:2048\n"
      "s_waitcnt lgkmcnt(8)\n"
      "v_mfma_f32_16x16x32_bf16 v[80:83], v[128:131], v[36:39], v[80:83]\n"
      "ds_read_b128 v[152:155], %[xa1] offset:4096\n"
      "v_mfma_f32_16x16x32_bf16 v[84:87], v[132:135], v[36:39], v[84:87]\n"
      "ds_read_b128 v[156:159], %[xa1] offset:6144\n"
      "v_add_u32 %[xa1], %[d1], %[xa1]\n"
      "v_mfma_f32_16x16x32_bf16 v[88:91], v[136:139], v[36:39], v[88:91]\n"
      "ds_read_b64_tr_b16 v[52:53], %[wa1] offset:26624\n"
      "v_mfma_f32_16x16x32_bf16 v[92:95], v[140:143], v[36:39], v[92:95]\n"
      "ds_read_b64_tr_b16 v[54:55], %[wa1] offset:27136\n"
      "s_waitcnt lgkmcnt(10)\n"
      "v_mfma_f32_16x16x32_bf16 v[96:99], v[128:131], v[40:43], v[96:99]\n"
      "ds_read_b64_tr_b16 v[56:57], %[wa2] offset:26624\n"
      "v_mfma_f32_16x16x32_bf16 v[100:103], v[132:135], v[40:43], v[100:103]\n"
      "ds_read_b64_tr_b16 v[58:59], %[wa2] offset:27136\n"
      "v_mfma_f32_16x16x32_bf16 v[104:107], v[136:139], v[40:43], v[104:107]\n"
      "ds_read_b64_tr_b16 v[60:61], %[wa3] offset:26624\n"
      "v_mfma_f32_16x16x32_bf16 v[108:111], v[140:143], v[40:43], v[108:111]\n"
      "ds_read_b64_tr_b16 v[62:63], %[wa3] offset:27136\n"
      "s_waitcnt lgkmcnt(12)\n"
      "v_mfma_f32_16x16x32_bf16 v[112:115], v[128:131], v[44:47], v[112:115]\n"
      "v_mfma_f32_16x16x32_bf16 v[116:119], v[132:135], v[44:47], v[116:119]\n"
      "v_mfma_f32_16x16x32_bf16 v[120:123], v[136:139], v[44:47], v[120:123]\n"
      "v_mfma_f32_16x16x32_bf16 v[124:127], v[140:143], v[44:47], v[124:127]\n"
      "s_waitcnt lgkmcnt(9)\n"
      "v_mfma_f32_16x16x32_bf16 v[64:67], v[144:147], v[48:51], v[64:67]\n"
      "s_waitcnt lgkmcnt(8)\n"
      "v_mfma_f32_16x16x32_bf16 v[68:71], v[148:151], v[48:51], v[68:71]\n"
      "s_waitcnt lgkmcnt(7)\n"
      "v_mfma_f32_16x16x32_bf16 v[72:75], v[152:155], v[48:51], v[72:75]\n"
      "s_waitcnt lgkmcnt(6)\n"
      "v_mfma_f32_16x16x32_bf16 v[76:79], v[156:159], v[48:51], v[76:79]\n"
      "s_waitcnt lgkmcnt(4)\n"
      "s_waitcnt lgkmcnt(0)\n"
      "s_barrier\n"
      "v_mfma_f32_16x16x32_bf16 v[80:83], v[144:147], v[52:55], v[80:83]\n"
      "ds_read_b64_tr_b16 v[32:33], %[wa0] offset:49152\n"
      "v_mfma_f32_16x16x32_bf16 v[84:87], v[148:151], v[52:55], v[84:87]\n"
      "ds_read_b64_tr_b16 v[34:35], %[wa0] offset:49664\n"
      "v_mfma_f32_16x16x32_bf16 v[88:91], v[152:155], v[52:55], v[88:91]\n"
      "ds_read_b128 v[128:131], %[xa0] offset:0\n"
      "v_mfma_f32_16x16x32_bf16 v[92:95], v[156:159], v[52:55], v[92:95]\n"
      "ds_read_b128 v[132:135], %[xa0] offset:2048\n"
      "v_mfma_f32_16x16x32_bf16 v[96:99], v[144:147], v[56:59], v[96:99]\n"
      "ds_read_b128 v[136:139], %[xa0] offset:4096\n"
      "v_mfma_f32_16x16x32_bf16 v[100:103], v[148:151], v[56:59], v[100:103]\n"
      "ds_read_b128 v[140:143], %[xa0] offset:6144\n"
      "v_add_u32 %[xa0], %[d2], %[xa0]\n"
      "v_mfma_f32_16x16x32_bf16 v[104:107], v[152:155], v[56:59], v[104:107]\n"
      "ds_read_b64_tr_b16 v[36:37], %[wa1] offset:49152\n"
      "v_mfma_f32_16x16x32_bf16 v[108:111], v[156:159], v[56:59], v[108:111]\n"
      "ds_read_b64_tr_b16 v[38:39], %[wa1] offset:49664\n"
      "v_mfma_f32_16x16x32_bf16 v[112:115], v[144:147], v[60:63], v[112:115]\n"
      "ds_read_b64_tr_b16 v[40:41], %[wa2] offset:49152\n"
      "v_mfma_f32_16x16x32_bf16 v[116:119], v[148:151], v[60:63], v[116:119]\n"
      "ds_read_b64_tr_b16 v[42:43], %[wa2] offset:49664\n"
      "v_mfma_f32_16x16x32_bf16 v[120:123], v[152:155], v[60:63], v[120:123]\n"
      "ds_read_b64_tr_b16 v[44:45], %[wa3] offset:49152\n"
      "v_mfma_f32_16x16x32_bf16 v[124:127], v[156:159], v[60:63], v[124:127]\n"
      "ds_read_b64_tr_b16 v[46:47], %[wa3] offset:49664\n"
      "s_waitcnt lgkmcnt(9)\n"
      "v_mfma_f32_16x16x32_bf16 v[64:67], v[128:131], v[32:35], v[64:67]\n"
      "ds_read_b64_tr_b16 v[48:49], %[wa0] offset:51200\n"
      "s_waitcnt lgkmcnt(9)\n"
      "v_mfma_f32_16x16x32_bf16 v[68:71], v[132:135], v[32:35], v[68:71]\n"
      "ds_read_b64_tr_b16 v[50:51], %[wa0] offset:51712\n"
      "s_waitcnt lgkmcnt(9)\n"
      "v_mfma_f32_16x16x32_bf16 v[72:75], v[136:139], v[32:35], v[72:75]\n"
      "ds_read_b128 v[144:147], %[xa1] offset:0\n"
      "s_waitcnt lgkmcnt(9)\n"
      "v_mfma_f32_16x16x32_bf16 v[76:79], v[140:143], v[32:35], v[76:79]\n"
      "ds_read_b128 v[148:151], %[xa1] offset:2048\n"
      "s_waitcnt lgkmcnt(8)\n"
      "v_mfma_f32_16x16x32_bf16 v[80:83], v[128:131], v[36:39], v[80:83]\n"
      "ds_read_b128 v[152:155], %[xa1] offset:4096\n"
      "v_mfma_f32_16x16x32_bf16 v[84:87], v[132:135], v[36:39], v[84:87]\n"
      "ds_read_b128 v[156:159], %[xa1] offset:6144\n"
      "v_add_u32 %[xa1], %[d2], %[xa1]\n"
      "v_mfma_f32_16x16x32_bf16 v[88:91], v[136:139], v[36:39], v[88:91]\n"
      "ds_read_b64_tr_b16 v[52:53], %[wa1] offset:51200\n"
      "v_mfma_f32_16x16x32_bf16 v[92:95], v[140:143], v[36:39], v[92:95]\n"
      "ds_read_b64_tr_b16 v[54:55], %[wa1] offset:51712\n"
      "s_waitcnt lgkmcnt(10)\n"
      "v_mfma_f32_16x16x32_bf16 v[96:99], v[128:131], v[40:43], v[96:99]\n"
      "ds_read_b64_tr_b16 v[56:57], %[wa2] offset:51200\n"
      "v_mfma_f32_16x16x32_bf16 v[100:103], v[132:135], v[40:43], v[100:103]\n"
      "ds_read_b64_tr_b16 v[58:59], %[wa2] offset:51712\n"
      "v_mfma_f32_16x16x32_bf16 v[104:107], v[136:139], v[40:43], v[104:107]\n"
      "ds_read_b64_tr_b16 v[60:61], %[wa3] offset:51200\n"
      "v_mfma_f32_16x16x32_bf16 v[108:111], v[140:143], v[40:43], v[108:111]\n"
      "ds_read_b64_tr_b16 v[62:63], %[wa3] offset:51712\n"
      "s_waitcnt lgkmcnt(12)\n"
      "v_mfma_f32_16x16x32_bf16 v[112:115], v[128:131], v[44:47], v[112:115]\n"
      "v_mfma_f32_16x16x32_bf16 v[116:119], v[132:135], v[44:47], v[116:119]\n"
      "v_mfma_f32_16x16x32_bf16 v[120:123], v[136:139], v[44:47], v[120:123]\n"
      "v_mfma_f32_16x16x32_bf16 v[124:127], v[140:143], v[44:47], v[124:127]\n"
      "s_waitcnt lgkmcnt(9)\n"
      "v_mfma_f32_16x16x32_bf16 v[64:67], v[144:147], v[48:51], v[64:67]\n"
      "s_waitcnt lgkmcnt(8)\n"
      "v_mfma_f32_16x16x32_bf16 v[68:71], v[148:151], v[48:51], v[68:71]\n"
      "s_waitcnt lgkmcnt(7)\n"
      "v_mfma_f32_16x16x32_bf16 v[72:75], v[152:155], v[48:51], v[72:75]\n"
      "s_waitcnt lgkmcnt(6)\n"
      "v_mfma_f32_16x16x32_bf16 v[76:79], v[156:159], v[48:51], v[76:79]\n"
      "s_waitcnt lgkmcnt(4)\n"
      "s_waitcnt lgkmcnt(0)\n"
      "s_barrier\n"
      "v_mfma_f32_16x16x32_bf16 v[80:83], v[144:147], v[52:55], v[80:83]\n"
      "ds_read_b64_tr_b16 v[32:33], %[wa0] offset:0\n"
      "v_mfma_f32_16x16x32_bf16 v[84:87], v[148:151], v[52:55], v[84:87]\n"
      "ds_read_b64_tr_b16 v[34:35], %[wa0] offset:512\n"
      "v_mfma_f32_16x16x32_bf16 v[88:91], v[152:155], v[52:55], v[88:91]\n"
      "ds_read_b128 v[128:131], %[xa0] offset:0\n"
      "v_mfma_f32_16x16x32_bf16 v[92:95], v[156:159], v[52:55], v[92:95]\n"
      "ds_read_b128 v[132:135], %[xa0] offset:2048\n"
      "v_mfma_f32_16x16x32_bf16 v[96:99], v[144:147], v[56:59], v[96:99]\n"
      "ds_read_b128 v[136:139], %[xa0] offset:4096\n"
      "v_mfma_f32_16x16x32_bf16 v[100:103], v[148:151], v[56:59], v[100:103]\n"
      "ds_read_b128 v[140:143], %[xa0] offset:6144\n"
      "v_add_u32 %[xa0], %[d0], %[xa0]\n"
      "v_mfma_f32_16x16x32_bf16 v[104:107], v[152:155], v[56:59], v[104:107]\n"
      "ds_read_b64_tr_b16 v[36:37], %[wa1] offset:0\n"
      "v_mfma_f32_16x16x32_bf16 v[108:111], v[156:159], v[56:59], v[108:111]\n"
      "ds_read_b64_tr_b16 v[38:39], %[wa1] offset:512\n"
      "v_mfma_f32_16x16x32_bf16 v[112:115], v[144:147], v[60:63], v[112:115]\n"
      "ds_read_b64_tr_b16 v[40:41], %[wa2] offset:0\n"
      "v_mfma_f32_16x16x32_bf16 v[116:119], v[148:151], v[60:63], v[116:119]\n"
      "ds_read_b64_tr_b16 v[42:43], %[wa2] offset:512\n"
      "v_mfma_f32_16x16x32_bf16 v[120:123], v[152:155], v[60:63], v[120:123]\n"
      "ds_read_b64_tr_b16 v[44:45], %[wa3] offset:0\n"
      "v_mfma_f32_16x16x32_bf16 v[124:127], v[156:159], v[60:63], v[124:127]\n"
      "ds_read_b64_tr_b16 v[46:47], %[wa3] offset:512\n"
      "s_waitcnt lgkmcnt(9)\n"
      "v_mfma_f32_16x16x32_bf16 v[64:67], v[128:131], v[32:35], v[64:67]\n"
      "ds_read_b64_tr_b16 v[48:49], %[wa0] offset:2048\n"
      "s_waitcnt lgkmcnt(9)\n"
      "v_mfma_f32_16x16x32_bf16 v[68:71], v[132:135], v[32:35], v[68:71]\n"
      "ds_read_b64_tr_b16 v[50:51], %[wa0] offset:2560\n"
      "s_waitcnt lgkmcnt(9)\n"
      "v_mfma_f32_16x16x32_bf16 v[72:75], v[136:139], v[32:35], v[72:75]\n"
      "ds_read_b128 v[144:147], %[xa1] offset:0\n"
      "s_waitcnt lgkmcnt(9)\n"
      "v_mfma_f32_16x16x32_bf16 v[76:79], v[140:143], v[32:35], v[76:79]\n"
      "ds_read_b128 v[148:151], %[xa1] offset:2048\n"
      "s_waitcnt lgkmcnt(8)\n"
      "v_mfma_f32_16x16x32_bf16 v[80:83], v[128:131], v[36:39], v[80:83]\n"
      "ds_read_b128 v[152:155], %[xa1] offset:4096\n"
      "v_mfma_f32_16x16x32_bf16 v[84:87], v[132:135], v[36:39], v[84:87]\n"
      "ds_read_b128 v[156:159], %[xa1] offset:6144\n"
      "v_add_u32 %[xa1], %[d0], %[xa1]\n"
      "v_mfma_f32_16x16x32_bf16 v[88:91], v[136:139], v[36:39], v[88:91]\n"
      "ds_read_b64_tr_b16 v[52:53], %[wa1] offset:2048\n"
      "v_mfma_f32_16x16x32_bf16 v[92:95], v[140:143], v[36:39], v[92:95]\n"
      "ds_read_b64_tr_b16 v[54:55], %[wa1] offset:2560\n"
      "s_waitcnt lgkmcnt(10)\n"
      "v_mfma_f32_16x16x32_bf16 v[96:99], v[128:131], v[40:43], v[96:99]\n"
      "ds_read_b64_tr_b16 v[56:57], %[wa2] offset:2048\n"
      "v_mfma_f32_16x16x32_bf16 v[100:103], v[132:135], v[40:43], v[100:103]\n"
      "ds_read_b64_tr_b16 v[58:59], %[wa2] offset:2560\n"
      "v_mfma_f32_16x16x32_bf16 v[104:107], v[136:139], v[40:43], v[104:107]\n"
      "ds_read_b64_tr_b16 v[60:61], %[wa3] offset:2048\n"
      "v_mfma_f32_16x16x32_bf16 v[108:111], v[140:143], v[40:43], v[108:111]\n"
      "ds_read_b64_tr_b16 v[62:63], %[wa3] offset:2560\n"
      "s_waitcnt lgkmcnt(12)\n"
      "v_mfma_f32_16x16x32_bf16 v[112:115], v[128:131], v[44:47], v[112:115]\n"
      "v_mfma_f32_16x16x32_bf16 v[116:119], v[132:135], v[44:47], v[116:119]\n"
      "v_mfma_f32_16x16x32_bf16 v[120:123], v[136:139], v[44:47], v[120:123]\n"
      "v_mfma_f32_16x16x32_bf16 v[124:127], v[140:143], v[44:47], v[124:127]\n"
      "s_waitcnt lgkmcnt(9)\n"
      "v_mfma_f32_16x16x32_bf16 v[64:67], v[144:147], v[48:51], v[64:67]\n"
      "s_waitcnt lgkmcnt(8)\n"
      "v_mfma_f32_16x16x32_bf16 v[68:71], v[148:151], v[48:51], v[68:71]\n"
      "s_waitcnt lgkmcnt(7)\n"
      "v_mfma_f32_16x16x32_bf16 v[72:75], v[152:155], v[48:51], v[72:75]\n"
      "s_waitcnt lgkmcnt(6)\n"
      "v_mfma_f32_16x16x32_bf16 v[76:79], v[156:159], v[48:51], v[76:79]\n"
      "s_waitcnt lgkmcnt(4)\n"
      "s_waitcnt lgkmcnt(0)\n"
      "s_barrier\n"
      "v_mfma_f32_16x16x32_bf16 v[80:83], v[144:147], v[52:55], v[80:83]\n"
      "ds_read_b64_tr_b16 v[32:33], %[wa0] offset:24576\n"
      "v_mfma_f32_16x16x32_bf16 v[84:87], v[148:151], v[52:55], v[84:87]\n"
      "ds_read_b64_tr_b16 v[34:35], %[wa0] offset:25088\n"
      "v_mfma_f32_16x16x32_bf16 v[88:91], v[152:155], v[52:55], v[88:91]\n"
      "ds_read_b128 v[128:131], %[xa0] offset:0\n"
      "v_mfma_f32_16x16x32_bf16 v[92:95], v[156:159], v[52:55], v[92:95]\n"
      "ds_read_b128 v[132:135], %[xa0] offset:2048\n"
      "v_mfma_f32_16x16x32_bf16 v[96:99], v[144:147], v[56:59], v[96:99]\n"
      "ds_read_b128 v[136:139], %[xa0] offset:4096\n"
      "v_mfma_f32_16x16x32_bf16 v[100:103], v[148:151], v[56:59], v[100:103]\n"
      "ds_read_b128 v[140:143], %[xa0] offset:6144\n"
      "v_add_u32 %[xa0], %[d1], %[xa0]\n"
      "v_mfma_f32_16x16x32_bf16 v[104:107], v[152:155], v[56:59], v[104:107]\n"
      "ds_read_b64_tr_b16 v[36:37], %[wa1] offset:24576\n"
      "v_mfma_f32_16x16x32_bf16 v[108:111], v[156:159], v[56:59], v[108:111]\n"
      "ds_read_b64_tr_b16 v[38:39], %[wa1] offset:25088\n"
      "v_mfma_f32_16x16x32_bf16 v[112:115], v[144:147], v[60:63], v[112:115]\n"
      "ds_read_b64_tr_b16 v[40:41], %[wa2] offset:24576\n"
      "v_mfma_f32_16x16x32_bf16 v[116:119], v[148:151], v[60:63], v[116:119]\n"
      "ds_read_b64_tr_b16 v[42:43], %[wa2] offset:25088\n"
      "v_mfma_f32_16x16x32_bf16 v[120:123], v[152:155], v[60:63], v[120:123]\n"
      "ds_read_b64_tr_b16 v[44:45], %[wa3] offset:24576\n"
      "v_mfma_f32_16x16x32_bf16 v[124:127], v[156:159], v[60:63], v[124:127]\n"
      "ds_read_b64_tr_b16 v[46:47], %[wa3] offset:25088\n"
      "s_waitcnt lgkmcnt(9)\n"
      "v_mfma_f32_16x16x32_bf16 v[64:67], v[128:131], v[32:35], v[64:67]\n"
      "ds_read_b64_tr_b16 v[48:49], %[wa0] offset:26624\n"
      "s_waitcnt lgkmcnt(9)\n"
      "v_mfma_f32_16x16x32_bf16 v[68:71], v[132:135], v[32:35], v[68:71]\n"
      "ds_read_b64_tr_b16 v[50:51], %[wa0] offset:27136\n"
      "s_waitcnt lgkmcnt(9)\n"
      "v_mfma_f32_16x16x32_bf16 v[72:75], v[136:139], v[32:35], v[72:75]\n"
      "ds_read_b128 v[144:147], %[xa1] offset:0\n"
      "s_waitcnt lgkmcnt(9)\n"
      "v_mfma_f32_16x16x32_bf16 v[76:79], v[140:143], v[32:35], v[76:79]\n"
      "ds_read_b128 v[148:151], %[xa1] offset:2048\n"
      "s_waitcnt lgkmcnt(8)\n"
      "v_mfma_f32_16x16x32_bf16 v[80:83], v[128:131], v[36:39], v[80:83]\n"
      "ds_read_b128 v[152:155], %[xa1] offset:4096\n"
      "v_mfma_f32_16x16x32_bf16 v[84:87], v[132:135], v[36:39], v[84:87]\n"
      "ds_read_b128 v[156:159], %[xa1] offset:6144\n"
      "v_add_u32 %[xa1], %[d1], %[xa1]\n"
      "v_mfma_f32_16x16x32_bf16 v[88:91], v[136:139], v[36:39], v[88:91]\n"
      "ds_read_b64_tr_b16 v[52:53], %[wa1] offset:26624\n"
      "v_mfma_f32_16x16x32_bf16 v[92:95], v[140:143], v[36:39], v[92:95]\n"
      "ds_read_b64_tr_b16 v[54:55], %[wa1] offset:27136\n"
      "s_waitcnt lgkmcnt(10)\n"
      "v_mfma_f32_16x16x32_bf16 v[96:99], v[128:131], v[40:43], v[96:99]\n"
      "ds_read_b64_tr_b16 v[56:57], %[wa2] offset:26624\n"
      "v_mfma_f32_16x16x32_bf16 v[100:103], v[132:135], v[40:43], v[100:103]\n"
      "ds_read_b64_tr_b16 v[58:59], %[wa2] offset:27136\n"
      "v_mfma_f32_16x16x32_bf16 v[104:107], v[136:139], v[40:43], v[104:107]\n"
      "ds_read_b64_tr_b16 v[60:61], %[wa3] offset:26624\n"
      "v_mfma_f32_16x16x32_bf16 v[108:111], v[140:143], v[40:43], v[108:111]\n"
      "ds_read_b64_tr_b16 v[62:63], %[wa3] offset:27136\n"
      "s_waitcnt lgkmcnt(12)\n"
      "v_mfma_f32_16x16x32_bf16 v[112:115], v[128:131], v[44:47], v[112:115]\n"
      "v_mfma_f32_16x16x32_bf16 v[116:119], v[132:135], v[44:47], v[116:119]\n"
      "v_mfma_f32_16x16x32_bf16 v[120:123], v[136:139], v[44:47], v[120:123]\n"
      "v_mfma_f32_16x16x32_bf16 v[124:127], v[140:143], v[44:47], v[124:127]\n"
      "s_waitcnt lgkmcnt(9)\n"
      "v_mfma_f32_16x16x32_bf16 v[64:67], v[144:147], v[48:51], v[64:67]\n"
      "s_waitcnt lgkmcnt(8)\n"
      "v_mfma_f32_16x16x32_bf16 v[68:71], v[148:151], v[48:51], v[68:71]\n"
      "s_waitcnt lgkmcnt(7)\n"
      "v_mfma_f32_16x16x32_bf16 v[72:75], v[152:155], v[48:51], v[72:75]\n"
      "s_waitcnt lgkmcnt(6)\n"
      "v_mfma_f32_16x16x32_bf16 v[76:79], v[156:159], v[48:51], v[76:79]\n"
      "s_waitcnt lgkmcnt(4)\n"
      "s_waitcnt lgkmcnt(0)\n"
      "s_barrier\n"
      "v_mfma_f32_16x16x32_bf16 v[80:83], v[144:147], v[52:55], v[80:83]\n"
      "ds_read_b64_tr_b16 v[32:33], %[wa0] offset:49152\n"
      "v_mfma_f32_16x16x32_bf16 v[84:87], v[148:151], v[52:55], v[84:87]\n"
      "ds_read_b64_tr_b16 v[34:35], %[wa0] offset:49664\n"
      "v_mfma_f32_16x16x32_bf16 v[88:91], v[152:155], v[52:55], v[88:91]\n"
      "ds_read_b128 v[128:131], %[xa0] offset:0\n"
      "v_mfma_f32_16x16x32_bf16 v[92:95], v[156:159], v[52:55], v[92:95]\n"
      "ds_read_b128 v[132:135], %[xa0] offset:2048\n"
      "v_mfma_f32_16x16x32_bf16 v[96:99], v[144:147], v[56:59], v[96:99]\n"
      "ds_read_b128 v[136:139], %[xa0] offset:4096\n"
      "v_mfma_f32_16x16x32_bf16 v[100:103], v[148:151], v[56:59], v[100:103]\n"
      "ds_read_b128 v[140:143], %[xa0] offset:6144\n"
      "v_add_u32 %[xa0], %[d2], %[xa0]\n"
      "v_mfma_f32_16x16x32_bf16 v[104:107], v[152:155], v[56:59], v[104:107]\n"
      "ds_read_b64_tr_b16 v[36:37], %[wa1] offset:49152\n"
      "v_mfma_f32_16x16x32_bf16 v[108:111], v[156:159], v[56:59], v[108:111]\n"
      "ds_read_b64_tr_b16 v[38:39], %[wa1] offset:49664\n"
      "v_mfma_f32_16x16x32_bf16 v[112:115], v[144:147], v[60:63], v[112:115]\n"
      "ds_read_b64_tr_b16 v[40:41], %[wa2] offset:49152\n"
      "v_mfma_f32_16x16x32_bf16 v[116:119], v[148:151], v[60:63], v[116:119]\n"
      "ds_read_b64_tr_b16 v[42:43], %[wa2] offset:49664\n"
      "v_mfma_f32_16x16x32_bf16 v[120:123], v[152:155], v[60:63], v[120:123]\n"
      "ds_read_b64_tr_b16 v[44:45], %[wa3] offset:49152\n"
      "v_mfma_f32_16x16x32_bf16 v[124:127], v[156:159], v[60:63], v[124:127]\n"
      "ds_read_b64_tr_b16 v[46:47], %[wa3] offset:49664\n"
      "s_waitcnt lgkmcnt(9)\n"
      "v_mfma_f32_16x16x32_bf16 v[64:67], v[128:131], v[32:35], v[64:67]\n"
      "ds_read_b64_tr_b16 v[48:49], %[wa0] offset:51200\n"
      "s_waitcnt lgkmcnt(9)\n"
      "v_mfma_f32_16x16x32_bf16 v[68:71], v[132:135], v[32:35], v[68:71]\n"
      "ds_read_b64_tr_b16 v[50:51], %[wa0] offset:51712\n"
      "s_waitcnt lgkmcnt(9)\n"
      "v_mfma_f32_16x16x32_bf16 v[72:75], v[136:139], v[32:35], v[72:75]\n"
      "ds_read_b128 v[144:147], %[xa1] offset:0\n"
      "s_waitcnt lgkmcnt(9)\n"
      "v_mfma_f32_16x16x32_bf16 v[76:79], v[140:143], v[32:35], v[76:79]\n"
      "ds_read_b128 v[148:151], %[xa1] offset:2048\n"
      "s_waitcnt lgkmcnt(8)\n"
      "v_mfma_f32_16x16x32_bf16 v[80:83], v[128:131], v[36:39], v[80:83]\n"
      "ds_read_b128 v[152:155], %[xa1] offset:4096\n"
      "v_mfma_f32_16x16x32_bf16 v[84:87], v[132:135], v[36:39], v[84:87]\n"
      "ds_read_b128 v[156:159], %[xa1] offset:6144\n"
      "v_add_u32 %[xa1], %[d2], %[xa1]\n"
      "v_mfma_f32_16x16x32_bf16 v[88:91], v[136:139], v[36:39], v[88:91]\n"
      "ds_read_b64_tr_b16 v[52:53], %[wa1] offset:51200\n"
      "v_mfma_f32_16x16x32_bf16 v[92:95], v[140:143], v[36:39], v[92:95]\n"
      "ds_read_b64_tr_b16 v[54:55], %[wa1] offset:51712\n"
      "s_waitcnt lgkmcnt(10)\n"
      "v_mfma_f32_16x16x32_bf16 v[96:99], v[128:131], v[40:43], v[96:99]\n"
      "ds_read_b64_tr_b16 v[56:57], %[wa2] offset:51200\n"
      "v_mfma_f32_16x16x32_bf16 v[100:103], v[132:135], v[40:43], v[100:103]\n"
      "ds_read_b64_tr_b16 v[58:59], %[wa2] offset:51712\n"
      "v_mfma_f32_16x16x32_bf16 v[104:107], v[136:139], v[40:43], v[104:107]\n"
      "ds_read_b64_tr_b16 v[60:61], %[wa3] offset:51200\n"
      "v_mfma_f32_16x16x32_bf16 v[108:111], v[140:143], v[40:43], v[108:111]\n"
      "ds_read_b64_tr_b16 v[62:63], %[wa3] offset:51712\n"
      "s_waitcnt lgkmcnt(12)\n"
      "v_mfma_f32_16x16x32_bf16 v[112:115], v[128:131], v[44:47], v[112:115]\n"
      "v_mfma_f32_16x16x32_bf16 v[116:119], v[132:135], v[44:47], v[116:119]\n"
      "v_mfma_f32_16x16x32_bf16 v[120:123], v[136:139], v[44:47], v[120:123]\n"
      "v_mfma_f32_16x16x32_bf16 v[124:127], v[140:143], v[44:47], v[124:127]\n"
      "s_waitcnt lgkmcnt(9)\n"
      "v_mfma_f32_16x16x32_bf16 v[64:67], v[144:147], v[48:51], v[64:67]\n"
      "s_waitcnt lgkmcnt(8)\n"
      "v_mfma_f32_16x16x32_bf16 v[68:71], v[148:151], v[48:51], v[68:71]\n"
      "s_waitcnt lgkmcnt(7)\n"
      "v_mfma_f32_16x16x32_bf16 v[72:75], v[152:155], v[48:51], v[72:75]\n"
      "s_waitcnt lgkmcnt(6)\n"
      "v_mfma_f32_16x16x32_bf16 v[76:79], v[156:159], v[48:51], v[76:79]\n"
      "s_waitcnt lgkmcnt(4)\n"
      "s_waitcnt lgkmcnt(0)\n"
      "s_barrier\n"
      "v_mfma_f32_16x16x32_bf16 v[80:83], v[144:147], v[52:55], v[80:83]\n"
      "ds_read_b64_tr_b16 v[32:33], %[wa0] offset:0\n"
      "v_mfma_f32_16x16x32_bf16 v[84:87], v[148:151], v[52:55], v[84:87]\n"
      "ds_read_b64_tr_b16 v[34:35], %[wa0] offset:512\n"
      "v_mfma_f32_16x16x32_bf16 v[88:91], v[152:155], v[52:55], v[88:91]\n"
      "ds_read_b128 v[128:131], %[xa0] offset:0\n"
      "v_mfma_f32_16x16x32_bf16 v[92:95], v[156:159], v[52:55], v[92:95]\n"
      "ds_read_b128 v[132:135], %[xa0] offset:2048\n"
      "v_mfma_f32_16x16x32_bf16 v[96:99], v[144:147], v[56:59], v[96:99]\n"
      "ds_read_b128 v[136:139], %[xa0] offset:4096\n"
      "v_mfma_f32_16x16x32_bf16 v[100:103], v[148:151], v[56:59], v[100:103]\n"
      "ds_read_b128 v[140:143], %[xa0] offset:6144\n"
      "v_add_u32 %[xa0], %[d0], %[xa0]\n"
      "v_mfma_f32_16x16x32_bf16 v[104:107], v[152:155], v[56:59], v[104:107]\n"
      "ds_read_b64_tr_b16 v[36:37], %[wa1] offset:0\n"
      "v_mfma_f32_16x16x32_bf16 v[108:111], v[156:159], v[56:59], v[108:111]\n"
      "ds_read_b64_tr_b16 v[38:39], %[wa1] offset:512\n"
      "v_mfma_f32_16x16x32_bf16 v[112:115], v[144:147], v[60:63], v[112:115]\n"
      "ds_read_b64_tr_b16 v[40:41], %[wa2] offset:0\n"
      "v_mfma_f32_16x16x32_bf16 v[116:119], v[148:151], v[60:63], v[116:119]\n"
      "ds_read_b64_tr_b16 v[42:43], %[wa2] offset:512\n"
      "v_mfma_f32_16x16x32_bf16 v[120:123], v[152:155], v[60:63], v[120:123]\n"
      "ds_read_b64_tr_b16 v[44:45], %[wa3] offset:0\n"
      "v_mfma_f32_16x16x32_bf16 v[124:127], v[156:159], v[60:63], v[124:127]\n"
      "ds_read_b64_tr_b16 v[46:47], %[wa3] offset:512\n"
      "s_waitcnt lgkmcnt(9)\n"
      "v_mfma_f32_16x16x32_bf16 v[64:67], v[128:131], v[32:35], v[64:67]\n"
      "ds_read_b64_tr_b16 v[48:49], %[wa0] offset:2048\n"
      "s_waitcnt lgkmcnt(9)\n"
      "v_mfma_f32_16x16x32_bf16 v[68:71], v[132:135], v[32:35], v[68:71]\n"
      "ds_read_b64_tr_b16 v[50:51], %[wa0] offset:2560\n"
      "s_waitcnt lgkmcnt(9)\n"
      "v_mfma_f32_16x16x32_bf16 v[72:75], v[136:139], v[32:35], v[72:75]\n"
      "ds_read_b128 v[144:147], %[xa1] offset:0\n"
      "s_waitcnt lgkmcnt(9)\n"
      "v_mfma_f32_16x16x32_bf16 v[76:79], v[140:143], v[32:35], v[76:79]\n"
      "ds_read_b128 v[148:151], %[xa1] offset:2048\n"
      "s_waitcnt lgkmcnt(8)\n"
      "v_mfma_f32_16x16x32_bf16 v[80:83], v[128:131], v[36:39], v[80:83]\n"
      "ds_read_b128 v[152:155], %[xa1] offset:4096\n"
      "v_mfma_f32_16x16x32_bf16 v[84:87], v[132:135], v[36:39], v[84:87]\n"
      "ds_read_b128 v[156:159], %[xa1] offset:6144\n"
      "v_add_u32 %[xa1], %[d0], %[xa1]\n"
      "v_mfma_f32_16x16x32_bf16 v[88:91], v[136:139], v[36:39], v[88:91]\n"
      "ds_read_b64_tr_b16 v[52:53], %[wa1] offset:2048\n"
      "v_mfma_f32_16x16x32_bf16 v[92:95], v[140:143], v[36:39], v[92:95]\n"
      "ds_read_b64_tr_b16 v[54:55], %[wa1] offset:2560\n"
      "s_waitcnt lgkmcnt(10)\n"
      "v_mfma_f32_16x16x32_bf16 v[96:99], v[128:131], v[40:43], v[96:99]\n"
      "ds_read_b64_tr_b16 v[56:57], %[wa2] offset:2048\n"
      "v_mfma_f32_16x16x32_bf16 v[100:103], v[132:135], v[40:43], v[100:103]\n"
      "ds_read_b64_tr_b16 v[58:59], %[wa2] offset:2560\n"
      "v_mfma_f32_16x16x32_bf16 v[104:107], v[136:139], v[40:43], v[104:107]\n"
      "ds_read_b64_tr_b16 v[60:61], %[wa3] offset:2048\n"
      "v_mfma_f32_16x16x32_bf16 v[108:111], v[140:143], v[40:43], v[108:111]\n"
      "ds_read_b64_tr_b16 v[62:63], %[wa3] offset:2560\n"
      "s_waitcnt lgkmcnt(12)\n"
      "v_mfma_f32_16x16x32_bf16 v[112:115], v[128:131], v[44:47], v[112:115]\n"
      "v_mfma_f32_16x16x32_bf16 v[116:119], v[132:135], v[44:47], v[116:119]\n"
      "v_mfma_f32_16x16x32_bf16 v[120:123], v[136:139], v[44:47], v[120:123]\n"
      "v_mfma_f32_16x16x32_bf16 v[124:127], v[140:143], v[44:47], v[124:127]\n"
      "s_waitcnt lgkmcnt(9)\n"
      "v_mfma_f32_16x16x32_bf16 v[64:67], v[144:147], v[48:51], v[64:67]\n"
      "s_waitcnt lgkmcnt(8)\n"
      "v_mfma_f32_16x16x32_bf16 v[68:71], v[148:151], v[48:51], v[68:71]\n"
      "s_waitcnt lgkmcnt(7)\n"
      "v_mfma_f32_16x16x32_bf16 v[72:75], v[152:155], v[48:51], v[72:75]\n"
      "s_waitcnt lgkmcnt(6)\n"
      "v_mfma_f32_16x16x32_bf16 v[76:79], v[156:159], v[48:51], v[76:79]\n"
      "s_waitcnt lgkmcnt(4)\n"
      "s_waitcnt lgkmcnt(0)\n"
      "s_barrier\n"
      "v_mfma_f32_16x16x32_bf16 v[80:83], v[144:147], v[52:55], v[80:83]\n"
      "v_mfma_f32_16x16x32_bf16 v[84:87], v[148:151], v[52:55], v[84:87]\n"
      "v_mfma_f32_16x16x32_bf16 v[88:91], v[152:155], v[52:55], v[88:91]\n"
      "v_mfma_f32_16x16x32_bf16 v[92:95], v[156:159], v[52:55], v[92:95]\n"
      "v_mfma_f32_16x16x32_bf16 v[96:99], v[144:147], v[56:59], v[96:99]\n"
      "v_mfma_f32_16x16x32_bf16 v[100:103], v[148:151], v[56:59], v[100:103]\n"
      "v_mfma_f32_16x16x32_bf16 v[104:107], v[152:155], v[56:59], v[104:107]\n"
      "v_mfma_f32_16x16x32_bf16 v[108:111], v[156:159], v[56:59], v[108:111]\n"
      "v_mfma_f32_16x16x32_bf16 v[112:115], v[144:147], v[60:63], v[112:115]\n"
      "v_mfma_f32_16x16x32_bf16 v[116:119], v[148:151], v[60:63], v[116:119]\n"
      "v_mfma_f32_16x16x32_bf16 v[120:123], v[152:155], v[60:63], v[120:123]\n"
      "v_mfma_f32_16x16x32_bf16 v[124:127], v[156:159], v[60:63], v[124:127]\n"
      "s_nop 7\n"
        : "=&{v[64:79]}"(acc[0]), "=&{v[80:95]}"(acc[1]), "=&{v[96:111]}"(acc[2]), "=&{v[112:127]}"(acc[3]), [xa0] "+v"(xa0), [xa1] "+v"(xa1)
        : [wa0] "v"(wq[0]), [wa1] "v"(wq[1]), [wa2] "v"(wq[2]), [wa3] "v"(wq[3]), [d0] "s"(d0), [d1] "s"(d0), [d2] "s"(d2)
        : "v32", "v33", "v34", "v35", "v36", "v37", "v38", "v39", "v40", "v41", "v42", "v43", "v44", "v45", "v46", "v47", "v48", "v49", "v50", "v51", "v52", "v53", "v54", "v55", "v56", "v57", "v58", "v59", "v60", "v61", "v62", "v63", "v128", "v129", "v130", "v131", "v132", "v133", "v134", "v135", "v136", "v137", "v138", "v139", "v140", "v141", "v142", "v143", "v144", "v145", "v146", "v147", "v148", "v149", "v150", "v151", "v152", "v153", "v154", "v155", "v156", "v157", "v158", "v159", "memory");
#pragma unroll
      for (int n = 0; n < 4; ++n) {
#pragma unroll
        for (int p = 0; p < 2; ++p)
          st16(rs, __builtin_bit_cast(u32x4, pack8(acc[n], p)), ea[n], WS_KV + (2 * rb + wm) * KV_PAIR + p * 3840);
      }
    }
  }
  {
    u32 xa0 = sm + xl, xa1 = xa0 ^ 64, wa[3];
#pragma unroll
    for (int n = 0; n < 3; ++n) {
      const u32 T = 3 * wid + n;
      wa[n] = sm + K1_RING + kg * 6144 + (T >> 2) * 1024 + r * 128 + (((T & 3) ^ sg) << 5) + cg * 8;
    }
    bias[2] = f32x4{bflo(bb[2][0]), bfhi(bb[2][0]), bflo(bb[2][1]), bfhi(bb[2][1])};
    f32x16 acc[3];
    asm volatile(
      "s_barrier\n"
      "ds_read_b64_tr_b16 v[32:33], %[wa0] offset:24576\n"
      "ds_read_b64_tr_b16 v[34:35], %[wa0] offset:25088\n"
      "ds_read_b128 v[128:131], %[xa0] offset:0\n"
      "ds_read_b128 v[132:135], %[xa0] offset:2048\n"
      "ds_read_b128 v[136:139], %[xa0] offset:4096\n"
      "ds_read_b128 v[140:143], %[xa0] offset:6144\n"
      "ds_read_b64_tr_b16 v[36:37], %[wa1] offset:24576\n"
      "ds_read_b64_tr_b16 v[38:39], %[wa1] offset:25088\n"
      "ds_read_b64_tr_b16 v[40:41], %[wa2] offset:24576\n"
      "ds_read_b64_tr_b16 v[42:43], %[wa2] offset:25088\n"
      "v_add_u32 %[xa0], 0x2000, %[xa0]\n"
      "s_waitcnt lgkmcnt(7)\n"
      "v_mfma_f32_16x16x32_bf16 v[64:67], v[32:35], v[128:131], %[b0]\n"
      "ds_read_b64_tr_b16 v[48:49], %[wa0] offset:27648\n"
      "s_waitcnt lgkmcnt(7)\n"
      "v_mfma_f32_16x16x32_bf16 v[68:71], v[32:35], v[132:135], %[b0]\n"
      "ds_read_b64_tr_b16 v[50:51], %[wa0] offset:28160\n"
      "s_waitcnt lgkmcnt(7)\n"
      "v_mfma_f32_16x16x32_bf16 v[72:75], v[32:35], v[136:139], %[b0]\n"
      "ds_read_b128 v[144:147], %[xa1] offset:0\n"
      "s_waitcnt lgkmcnt(7)\n"
      "v_mfma_f32_16x16x32_bf16 v[76:79], v[32:35], v[140:143], %[b0]\n"
      "ds_read_b128 v[148:151], %[xa1] offset:2048\n"
      "s_waitcnt lgkmcnt(6)\n"
      "v_mfma_f32_16x16x32_bf16 v[80:83], v[36:39], v[128:131], %[b1]\n"
      "ds_read_b128 v[152:155], %[xa1] offset:4096\n"
      "v_mfma_f32_16x16x32_bf16 v[84:87], v[36:39], v[132:135], %[b1]\n"
      "ds_read_b128 v[156:159], %[xa1] offset:6144\n"
      "v_add_u32 %[xa1], 0x2000, %[xa1]\n"
      "v_mfma_f32_16x16x32_bf16 v[88:91], v[36:39], v[136:139], %[b1]\n"
      "ds_read_b64_tr_b16 v[52:53], %[wa1] offset:27648\n"
      "v_mfma_f32_16x16x32_bf16 v[92:95], v[36:39], v[140:143], %[b1]\n"
      "ds_read_b64_tr_b16 v[54:55], %[wa1] offset:28160\n"
      "s_waitcnt lgkmcnt(8)\n"
      "v_mfma_f32_16x16x32_bf16 v[96:99], v[40:43], v[128:131], %[b2]\n"
      "ds_read_b64_tr_b16 v[56:57], %[wa2] offset:27648\n"
      "v_mfma_f32_16x16x32_bf16 v[100:103], v[40:43], v[132:135], %[b2]\n"
      "ds_read_b64_tr_b16 v[58:59], %[wa2] offset:28160\n"
      "v_mfma_f32_16x16x32_bf16 v[104:107], v[40:43], v[136:139], %[b2]\n"
      "v_mfma_f32_16x16x32_bf16 v[108:111], v[40:43], v[140:143], %[b2]\n"
      "s_waitcnt lgkmcnt(7)\n"
      "v_mfma_f32_16x16x32_bf16 v[64:67], v[48:51], v[144:147], v[64:67]\n"
      "s_waitcnt lgkmcnt(6)\n"
      "v_mfma_f32_16x16x32_bf16 v[68:71], v[48:51], v[148:151], v[68:71]\n"
      "s_waitcnt lgkmcnt(5)\n"
      "v_mfma_f32_16x16x32_bf16 v[72:75], v[48:51], v[152:155], v[72:75]\n"
      "s_waitcnt lgkmcnt(4)\n"
      "v_mfma_f32_16x16x32_bf16 v[76:79], v[48:51], v[156:159], v[76:79]\n"
      "s_waitcnt lgkmcnt(2)\n"
      "s_waitcnt lgkmcnt(0)\n"
      "s_barrier\n"
      "v_mfma_f32_16x16x32_bf16 v[80:83], v[52:55], v[144:147], v[80:83]\n"
      "ds_read_b64_tr_b16 v[32:33], %[wa0] offset:49152\n"
      "ds_read_b64_tr_b16 v[34:35], %[wa0] offset:49664\n"
      "v_mfma_f32_16x16x32_bf16 v[84:87], v[52:55], v[148:151], v[84:87]\n"
      "ds_read_b128 v[128:131], %[xa0] offset:0\n"
      "ds_read_b128 v[132:135], %[xa0] offset:2048\n"
      "v_mfma_f32_16x16x32_bf16 v[88:91], v[52:55], v[152:155], v[88:91]\n"
      "ds_read_b128 v[136:139], %[xa0] offset:4096\n"
      "v_mfma_f32_16x16x32_bf16 v[92:95], v[52:55], v[156:159], v[92:95]\n"
      "ds_read_b128 v[140:143], %[xa0] offset:6144\n"
      "v_add_u32 %[xa0], 0x2000, %[xa0]\n"
      "v_mfma_f32_16x16x32_bf16 v[96:99], v[56:59], v[144:147], v[96:99]\n"
      "ds_read_b64_tr_b16 v[36:37], %[wa1] offset:49152\n"
      "v_mfma_f32_16x16x32_bf16 v[100:103], v[56:59], v[148:151], v[100:103]\n"
      "ds_read_b64_tr_b16 v[38:39], %[wa1] offset:49664\n"
      "v_mfma_f32_16x16x32_bf16 v[104:107], v[56:59], v[152:155], v[104:107]\n"
      "ds_read_b64_tr_b16 v[40:41], %[wa2] offset:49152\n"
      "v_mfma_f32_16x16x32_bf16 v[108:111], v[56:59], v[156:159], v[108:111]\n"
      "ds_read_b64_tr_b16 v[42:43], %[wa2] offset:49664\n"
      "s_waitcnt lgkmcnt(7)\n"
      "v_mfma_f32_16x16x32_bf16 v[64:67], v[32:35], v[128:131], v[64:67]\n"
      "ds_read_b64_tr_b16 v[48:49], %[wa0] offset:52224\n"
      "s_waitcnt lgkmcnt(7)\n"
      "v_mfma_f32_16x16x32_bf16 v[68:71], v[32:35], v[132:135], v[68:71]\n"
      "ds_read_b64_tr_b16 v[50:51], %[wa0] offset:52736\n"
      "s_waitcnt lgkmcnt(7)\n"
      "v_mfma_f32_16x16x32_bf16 v[72:75], v[32:35], v[136:139], v[72:75]\n"
      "ds_read_b128 v[144:147], %[xa1] offset:0\n"
      "s_waitcnt lgkmcnt(7)\n"
      "v_mfma_f32_16x16x32_bf16 v[76:79], v[32:35], v[140:143], v[76:79]\n"
      "ds_read_b128 v[148:151], %[xa1] offset:2048\n"
      "s_waitcnt lgkmcnt(6)\n"
      "v_mfma_f32_16x16x32_bf16 v[80:83], v[36:39], v[128:131], v[80:83]\n"
      "ds_read_b128 v[152:155], %[xa1] offset:4096\n"
      "v_mfma_f32_16x16x32_bf16 v[84:87], v[36:39], v[132:135], v[84:87]\n"
      "ds_read_b128 v[156:159], %[xa1] offset:6144\n"
      "v_add_u32 %[xa1], 0x2000, %[xa1]\n"
      "v_mfma_f32_16x16x32_bf16 v[88:91], v[36:39], v[136:139], v[88:91]\n"
      "ds_read_b64_tr_b16 v[52:53], %[wa1] offset:52224\n"
      "v_mfma_f32_16x16x32_bf16 v[92:95], v[36:39], v[140:143], v[92:95]\n"
      "ds_read_b64_tr_b16 v[54:55], %[wa1] offset:52736\n"
      "s_waitcnt lgkmcnt(8)\n"
      "v_mfma_f32_16x16x32_bf16 v[96:99], v[40:43], v[128:131], v[96:99]\n"
      "ds_read_b64_tr_b16 v[56:57], %[wa2] offset:52224\n"
      "v_mfma_f32_16x16x32_bf16 v[100:103], v[40:43], v[132:135], v[100:103]\n"
      "ds_read_b64_tr_b16 v[58:59], %[wa2] offset:52736\n"
      "v_mfma_f32_16x16x32_bf16 v[104:107], v[40:43], v[136:139], v[104:107]\n"
      "v_mfma_f32_16x16x32_bf16 v[108:111], v[40:43], v[140:143], v[108:111]\n"
      "s_waitcnt lgkmcnt(7)\n"
      "v_mfma_f32_16x16x32_bf16 v[64:67], v[48:51], v[144:147], v[64:67]\n"
      "s_waitcnt lgkmcnt(6)\n"
      "v_mfma_f32_16x16x32_bf16 v[68:71], v[48:51], v[148:151], v[68:71]\n"
      "s_waitcnt lgkmcnt(5)\n"
      "v_mfma_f32_16x16x32_bf16 v[72:75], v[48:51], v[152:155], v[72:75]\n"
      "s_waitcnt lgkmcnt(4)\n"
      "v_mfma_f32_16x16x32_bf16 v[76:79], v[48:51], v[156:159], v[76:79]\n"
      "s_waitcnt lgkmcnt(2)\n"
      "s_waitcnt lgkmcnt(0)\n"
      "s_waitcnt vmcnt(0)\n"
      "s_barrier\n"
      "v_mfma_f32_16x16x32_bf16 v[80:83], v[52:55], v[144:147], v[80:83]\n"
      "ds_read_b64_tr_b16 v[32:33], %[wa0] offset:0\n"
      "ds_read_b64_tr_b16 v[34:35], %[wa0] offset:512\n"
      "v_mfma_f32_16x16x32_bf16 v[84:87], v[52:55], v[148:151], v[84:87]\n"
      "ds_read_b128 v[128:131], %[xa0] offset:0\n"
      "ds_read_b128 v[132:135], %[xa0] offset:2048\n"
      "v_mfma_f32_16x16x32_bf16 v[88:91], v[52:55], v[152:155], v[88:91]\n"
      "ds_read_b128 v[136:139], %[xa0] offset:4096\n"
      "v_mfma_f32_16x16x32_bf16 v[92:95], v[52:55], v[156:159], v[92:95]\n"
      "ds_read_b128 v[140:143], %[xa0] offset:6144\n"
      "v_add_u32 %[xa0], 0x2000, %[xa0]\n"
      "v_mfma_f32_16x16x32_bf16 v[96:99], v[56:59], v[144:147], v[96:99]\n"
      "ds_read_b64_tr_b16 v[36:37], %[wa1] offset:0\n"
      "v_mfma_f32_16x16x32_bf16 v[100:103], v[56:59], v[148:151], v[100:103]\n"
      "ds_read_b64_tr_b16 v[38:39], %[wa1] offset:512\n"
      "v_mfma_f32_16x16x32_bf16 v[104:107], v[56:59], v[152:155], v[104:107]\n"
      "ds_read_b64_tr_b16 v[40:41], %[wa2] offset:0\n"
      "v_mfma_f32_16x16x32_bf16 v[108:111], v[56:59], v[156:159], v[108:111]\n"
      "ds_read_b64_tr_b16 v[42:43], %[wa2] offset:512\n"
      "s_waitcnt lgkmcnt(7)\n"
      "v_mfma_f32_16x16x32_bf16 v[64:67], v[32:35], v[128:131], v[64:67]\n"
      "ds_read_b64_tr_b16 v[48:49], %[wa0] offset:3072\n"
      "s_waitcnt lgkmcnt(7)\n"
      "v_mfma_f32_16x16x32_bf16 v[68:71], v[32:35], v[132:135], v[68:71]\n"
      "ds_read_b64_tr_b16 v[50:51], %[wa0] offset:3584\n"
      "s_waitcnt lgkmcnt(7)\n"
      "v_mfma_f32_16x16x32_bf16 v[72:75], v[32:35], v[136:139], v[72:75]\n"
      "ds_read_b128 v[144:147], %[xa1] offset:0\n"
      "s_waitcnt lgkmcnt(7)\n"
      "v_mfma_f32_16x16x32_bf16 v[76:79], v[32:35], v[140:143], v[76:79]\n"
      "ds_read_b128 v[148:151], %[xa1] offset:2048\n"
      "s_waitcnt lgkmcnt(6)\n"
      "v_mfma_f32_16x16x32_bf16 v[80:83], v[36:39], v[128:131], v[80:83]\n"
      "ds_read_b128 v[152:155], %[xa1] offset:4096\n"
      "v_mfma_f32_16x16x32_bf16 v[84:87], v[36:39], v[132:135], v[84:87]\n"
      "ds_read_b128 v[156:159], %[xa1] offset:6144\n"
      "v_add_u32 %[xa1], 0x2000, %[xa1]\n"
      "v_mfma_f32_16x16x32_bf16 v[88:91], v[36:39], v[136:139], v[88:91]\n"
      "ds_read_b64_tr_b16 v[52:53], %[wa1] offset:3072\n"
      "v_mfma_f32_16x16x32_bf16 v[92:95], v[36:39], v[140:143], v[92:95]\n"
      "ds_read_b64_tr_b16 v[54:55], %[wa1] offset:3584\n"
      "s_waitcnt lgkmcnt(8)\n"
      "v_mfma_f32_16x16x32_bf16 v[96:99], v[40:43], v[128:131], v[96:99]\n"
      "ds_read_b64_tr_b16 v[56:57], %[wa2] offset:3072\n"
      "v_mfma_f32_16x16x32_bf16 v[100:103], v[40:43], v[132:135], v[100:103]\n"
      "ds_read_b64_tr_b16 v[58:59], %[wa2] offset:3584\n"
      "v_mfma_f32_16x16x32_bf16 v[104:107], v[40:43], v[136:139], v[104:107]\n"
      "v_mfma_f32_16x16x32_bf16 v[108:111], v[40:43], v[140:143], v[108:111]\n"
      "s_waitcnt lgkmcnt(7)\n"
      "v_mfma_f32_16x16x32_bf16 v[64:67], v[48:51], v[144:147], v[64:67]\n"
      "s_waitcnt lgkmcnt(6)\n"
      "v_mfma_f32_16x16x32_bf16 v[68:71], v[48:51], v[148:151], v[68:71]\n"
      "s_waitcnt lgkmcnt(5)\n"
      "v_mfma_f32_16x16x32_bf16 v[72:75], v[48:51], v[152:155], v[72:75]\n"
      "s_waitcnt lgkmcnt(4)\n"
      "v_mfma_f32_16x16x32_bf16 v[76:79], v[48:51], v[156:159], v[76:79]\n"
      "s_waitcnt lgkmcnt(2)\n"
      "s_waitcnt lgkmcnt(0)\n"
      "s_barrier\n"
      "v_mfma_f32_16x16x32_bf16 v[80:83], v[52:55], v[144:147], v[80:83]\n"
      "ds_read_b64_tr_b16 v[32:33], %[wa0] offset:24576\n"
      "ds_read_b64_tr_b16 v[34:35], %[wa0] offset:25088\n"
      "v_mfma_f32_16x16x32_bf16 v[84:87], v[52:55], v[148:151], v[84:87]\n"
      "ds_read_b128 v[128:131], %[xa0] offset:0\n"
      "ds_read_b128 v[132:135], %[xa0] offset:2048\n"
      "v_mfma_f32_16x16x32_bf16 v[88:91], v[52:55], v[152:155], v[88:91]\n"
      "ds_read_b128 v[136:139], %[xa0] offset:4096\n"
      "v_mfma_f32_16x16x32_bf16 v[92:95], v[52:55], v[156:159], v[92:95]\n"
      "ds_read_b128 v[140:143], %[xa0] offset:6144\n"
      "v_add_u32 %[xa0], 0x2000, %[xa0]\n"
      "v_mfma_f32_16x16x32_bf16 v[96:99], v[56:59], v[144:147], v[96:99]\n"
      "ds_read_b64_tr_b16 v[36:37], %[wa1] offset:24576\n"
      "v_mfma_f32_16x16x32_bf16 v[100:103], v[56:59], v[148:151], v[100:103]\n"
      "ds_read_b64_tr_b16 v[38:39], %[wa1] offset:25088\n"
      "v_mfma_f32_16x16x32_bf16 v[104:107], v[56:59], v[152:155], v[104:107]\n"
      "ds_read_b64_tr_b16 v[40:41], %[wa2] offset:24576\n"
      "v_mfma_f32_16x16x32_bf16 v[108:111], v[56:59], v[156:159], v[108:111]\n"
      "ds_read_b64_tr_b16 v[42:43], %[wa2] offset:25088\n"
      "s_waitcnt lgkmcnt(7)\n"
      "v_mfma_f32_16x16x32_bf16 v[64:67], v[32:35], v[128:131], v[64:67]\n"
      "ds_read_b64_tr_b16 v[48:49], %[wa0] offset:27648\n"
      "s_waitcnt lgkmcnt(7)\n"
      "v_mfma_f32_16x16x32_bf16 v[68:71], v[32:35], v[132:135], v[68:71]\n"
      "ds_read_b64_tr_b16 v[50:51], %[wa0] offset:28160\n"
      "s_waitcnt lgkmcnt(7)\n"
      "v_mfma_f32_16x16x32_bf16 v[72:75], v[32:35], v[136:139], v[72:75]\n"
      "ds_read_b128 v[144:147], %[xa1] offset:0\n"
      "s_waitcnt lgkmcnt(7)\n"
      "v_mfma_f32_16x16x32_bf16 v[76:79], v[32:35], v[140:143], v[76:79]\n"
      "ds_read_b128 v[148:151], %[xa1] offset:2048\n"
      "s_waitcnt lgkmcnt(6)\n"
      "v_mfma_f32_16x16x32_bf16 v[80:83], v[36:39], v[128:131], v[80:83]\n"
      "ds_read_b128 v[152:155], %[xa1] offset:4096\n"
      "v_mfma_f32_16x16x32_bf16 v[84:87], v[36:39], v[132:135], v[84:87]\n"
      "ds_read_b128 v[156:159], %[xa1] offset:6144\n"
      "v_add_u32 %[xa1], 0x2000, %[xa1]\n"
      "v_mfma_f32_16x16x32_bf16 v[88:91], v[36:39], v[136:139], v[88:91]\n"
      "ds_read_b64_tr_b16 v[52:53], %[wa1] offset:27648\n"
      "v_mfma_f32_16x16x32_bf16 v[92:95], v[36:39], v[140:143], v[92:95]\n"
      "ds_read_b64_tr_b16 v[54:55], %[wa1] offset:28160\n"
      "s_waitcnt lgkmcnt(8)\n"
      "v_mfma_f32_16x16x32_bf16 v[96:99], v[40:43], v[128:131], v[96:99]\n"
      "ds_read_b64_tr_b16 v[56:57], %[wa2] offset:27648\n"
      "v_mfma_f32_16x16x32_bf16 v[100:103], v[40:43], v[132:135], v[100:103]\n"
      "ds_read_b64_tr_b16 v[58:59], %[wa2] offset:28160\n"
      "v_mfma_f32_16x16x32_bf16 v[104:107], v[40:43], v[136:139], v[104:107]\n"
      "v_mfma_f32_16x16x32_bf16 v[108:111], v[40:43], v[140:143], v[108:111]\n"
      "s_waitcnt lgkmcnt(7)\n"
      "v_mfma_f32_16x16x32_bf16 v[64:67], v[48:51], v[144:147], v[64:67]\n"
      "s_waitcnt lgkmcnt(6)\n"
      "v_mfma_f32_16x16x32_bf16 v[68:71], v[48:51], v[148:151], v[68:71]\n"
      "s_waitcnt lgkmcnt(5)\n"
      "v_mfma_f32_16x16x32_bf16 v[72:75], v[48:51], v[152:155], v[72:75]\n"
      "s_waitcnt lgkmcnt(4)\n"
      "v_mfma_f32_16x16x32_bf16 v[76:79], v[48:51], v[156:159], v[76:79]\n"
      "s_waitcnt lgkmcnt(2)\n"
      "s_waitcnt lgkmcnt(0)\n"
      "s_barrier\n"
      "v_mfma_f32_16x16x32_bf16 v[80:83], v[52:55], v[144:147], v[80:83]\n"
      "ds_read_b64_tr_b16 v[32:33], %[wa0] offset:49152\n"
      "ds_read_b64_tr_b16 v[34:35], %[wa0] offset:49664\n"
      "v_mfma_f32_16x16x32_bf16 v[84:87], v[52:55], v[148:151], v[84:87]\n"
      "ds_read_b128 v[128:131], %[xa0] offset:0\n"
      "ds_read_b128 v[132:135], %[xa0] offset:2048\n"
      "v_mfma_f32_16x16x32_bf16 v[88:91], v[52:55], v[152:155], v[88:91]\n"
      "ds_read_b128 v[136:139], %[xa0] offset:4096\n"
      "v_mfma_f32_16x16x32_bf16 v[92:95], v[52:55], v[156:159], v[92:95]\n"
      "ds_read_b128 v[140:143], %[xa0] offset:6144\n"
      "v_add_u32 %[xa0], 0x2000, %[xa0]\n"
      "v_mfma_f32_16x16x32_bf16 v[96:99], v[56:59], v[144:147], v[96:99]\n"
      "ds_read_b64_tr_b16 v[36:37], %[wa1] offset:49152\n"
      "v_mfma_f32_16x16x32_bf16 v[100:103], v[56:59], v[148:151], v[100:103]\n"
      "ds_read_b64_tr_b16 v[38:39], %[wa1] offset:49664\n"
      "v_mfma_f32_16x16x32_bf16 v[104:107], v[56:59], v[152:155], v[104:107]\n"
      "ds_read_b64_tr_b16 v[40:41], %[wa2] offset:49152\n"
      "v_mfma_f32_16x16x32_bf16 v[108:111], v[56:59], v[156:159], v[108:111]\n"
      "ds_read_b64_tr_b16 v[42:43], %[wa2] offset:49664\n"
      "s_waitcnt lgkmcnt(7)\n"
      "v_mfma_f32_16x16x32_bf16 v[64:67], v[32:35], v[128:131], v[64:67]\n"
      "ds_read_b64_tr_b16 v[48:49], %[wa0] offset:52224\n"
      "s_waitcnt lgkmcnt(7)\n"
      "v_mfma_f32_16x16x32_bf16 v[68:71], v[32:35], v[132:135], v[68:71]\n"
      "ds_read_b64_tr_b16 v[50:51], %[wa0] offset:52736\n"
      "s_waitcnt lgkmcnt(7)\n"
      "v_mfma_f32_16x16x32_bf16 v[72:75], v[32:35], v[136:139], v[72:75]\n"
      "ds_read_b128 v[144:147], %[xa1] offset:0\n"
      "s_waitcnt lgkmcnt(7)\n"
      "v_mfma_f32_16x16x32_bf16 v[76:79], v[32:35], v[140:143], v[76:79]\n"
      "ds_read_b128 v[148:151], %[xa1] offset:2048\n"
      "s_waitcnt lgkmcnt(6)\n"
      "v_mfma_f32_16x16x32_bf16 v[80:83], v[36:39], v[128:131], v[80:83]\n"
      "ds_read_b128 v[152:155], %[xa1] offset:4096\n"
      "v_mfma_f32_16x16x32_bf16 v[84:87], v[36:39], v[132:135], v[84:87]\n"
      "ds_read_b128 v[156:159], %[xa1] offset:6144\n"
      "v_add_u32 %[xa1], 0x2000, %[xa1]\n"
      "v_mfma_f32_16x16x32_bf16 v[88:91], v[36:39], v[136:139], v[88:91]\n"
      "ds_read_b64_tr_b16 v[52:53], %[wa1] offset:52224\n"
      "v_mfma_f32_16x16x32_bf16 v[92:95], v[36:39], v[140:143], v[92:95]\n"
      "ds_read_b64_tr_b16 v[54:55], %[wa1] offset:52736\n"
      "s_waitcnt lgkmcnt(8)\n"
      "v_mfma_f32_16x16x32_bf16 v[96:99], v[40:43], v[128:131], v[96:99]\n"
      "ds_read_b64_tr_b16 v[56:57], %[wa2] offset:52224\n"
      "v_mfma_f32_16x16x32_bf16 v[100:103], v[40:43], v[132:135], v[100:103]\n"
      "ds_read_b64_tr_b16 v[58:59], %[wa2] offset:52736\n"
      "v_mfma_f32_16x16x32_bf16 v[104:107], v[40:43], v[136:139], v[104:107]\n"
      "v_mfma_f32_16x16x32_bf16 v[108:111], v[40:43], v[140:143], v[108:111]\n"
      "s_waitcnt lgkmcnt(7)\n"
      "v_mfma_f32_16x16x32_bf16 v[64:67], v[48:51], v[144:147], v[64:67]\n"
      "s_waitcnt lgkmcnt(6)\n"
      "v_mfma_f32_16x16x32_bf16 v[68:71], v[48:51], v[148:151], v[68:71]\n"
      "s_waitcnt lgkmcnt(5)\n"
      "v_mfma_f32_16x16x32_bf16 v[72:75], v[48:51], v[152:155], v[72:75]\n"
      "s_waitcnt lgkmcnt(4)\n"
      "v_mfma_f32_16x16x32_bf16 v[76:79], v[48:51], v[156:159], v[76:79]\n"
      "s_waitcnt lgkmcnt(2)\n"
      "s_waitcnt lgkmcnt(0)\n"
      "s_barrier\n"
      "v_mfma_f32_16x16x32_bf16 v[80:83], v[52:55], v[144:147], v[80:83]\n"
      "ds_read_b64_tr_b16 v[32:33], %[wa0] offset:0\n"
      "ds_read_b64_tr_b16 v[34:35], %[wa0] offset:512\n"
      "v_mfma_f32_16x16x32_bf16 v[84:87], v[52:55], v[148:151], v[84:87]\n"
      "ds_read_b128 v[128:131], %[xa0] offset:0\n"
      "ds_read_b128 v[132:135], %[xa0] offset:2048\n"
      "v_mfma_f32_16x16x32_bf16 v[88:91], v[52:55], v[152:155], v[88:91]\n"
      "ds_read_b128 v[136:139], %[xa0] offset:4096\n"
      "v_mfma_f32_16x16x32_bf16 v[92:95], v[52:55], v[156:159], v[92:95]\n"
      "ds_read_b128 v[140:143], %[xa0] offset:6144\n"
      "v_add_u32 %[xa0], 0x2000, %[xa0]\n"
      "v_mfma_f32_16x16x32_bf16 v[96:99], v[56:59], v[144:147], v[96:99]\n"
      "ds_read_b64_tr_b16 v[36:37], %[wa1] offset:0\n"
      "v_mfma_f32_16x16x32_bf16 v[100:103], v[56:59], v[148:151], v[100:103]\n"
      "ds_read_b64_tr_b16 v[38:39], %[wa1] offset:512\n"
      "v_mfma_f32_16x16x32_bf16 v[104:107], v[56:59], v[152:155], v[104:107]\n"
      "ds_read_b64_tr_b16 v[40:41], %[wa2] offset:0\n"
      "v_mfma_f32_16x16x32_bf16 v[108:111], v[56:59], v[156:159], v[108:111]\n"
      "ds_read_b64_tr_b16 v[42:43], %[wa2] offset:512\n"
      "s_waitcnt lgkmcnt(7)\n"
      "v_mfma_f32_16x16x32_bf16 v[64:67], v[32:35], v[128:131], v[64:67]\n"
      "ds_read_b64_tr_b16 v[48:49], %[wa0] offset:3072\n"
      "s_waitcnt lgkmcnt(7)\n"
      "v_mfma_f32_16x16x32_bf16 v[68:71], v[32:35], v[132:135], v[68:71]\n"
      "ds_read_b64_tr_b16 v[50:51], %[wa0] offset:3584\n"
      "s_waitcnt lgkmcnt(7)\n"
      "v_mfma_f32_16x16x32_bf16 v[72:75], v[32:35], v[136:139], v[72:75]\n"
      "ds_read_b128 v[144:147], %[xa1] offset:0\n"
      "s_waitcnt lgkmcnt(7)\n"
      "v_mfma_f32_16x16x32_bf16 v[76:79], v[32:35], v[140:143], v[76:79]\n"
      "ds_read_b128 v[148:151], %[xa1] offset:2048\n"
      "s_waitcnt lgkmcnt(6)\n"
      "v_mfma_f32_16x16x32_bf16 v[80:83], v[36:39], v[128:131], v[80:83]\n"
      "ds_read_b128 v[152:155], %[xa1] offset:4096\n"
      "v_mfma_f32_16x16x32_bf16 v[84:87], v[36:39], v[132:135], v[84:87]\n"
      "ds_read_b128 v[156:159], %[xa1] offset:6144\n"
      "v_add_u32 %[xa1], 0x2000, %[xa1]\n"
      "v_mfma_f32_16x16x32_bf16 v[88:91], v[36:39], v[136:139], v[88:91]\n"
      "ds_read_b64_tr_b16 v[52:53], %[wa1] offset:3072\n"
      "v_mfma_f32_16x16x32_bf16 v[92:95], v[36:39], v[140:143], v[92:95]\n"
      "ds_read_b64_tr_b16 v[54:55], %[wa1] offset:3584\n"
      "s_waitcnt lgkmcnt(8)\n"
      "v_mfma_f32_16x16x32_bf16 v[96:99], v[40:43], v[128:131], v[96:99]\n"
      "ds_read_b64_tr_b16 v[56:57], %[wa2] offset:3072\n"
      "v_mfma_f32_16x16x32_bf16 v[100:103], v[40:43], v[132:135], v[100:103]\n"
      "ds_read_b64_tr_b16 v[58:59], %[wa2] offset:3584\n"
      "v_mfma_f32_16x16x32_bf16 v[104:107], v[40:43], v[136:139], v[104:107]\n"
      "v_mfma_f32_16x16x32_bf16 v[108:111], v[40:43], v[140:143], v[108:111]\n"
      "s_waitcnt lgkmcnt(7)\n"
      "v_mfma_f32_16x16x32_bf16 v[64:67], v[48:51], v[144:147], v[64:67]\n"
      "s_waitcnt lgkmcnt(6)\n"
      "v_mfma_f32_16x16x32_bf16 v[68:71], v[48:51], v[148:151], v[68:71]\n"
      "s_waitcnt lgkmcnt(5)\n"
      "v_mfma_f32_16x16x32_bf16 v[72:75], v[48:51], v[152:155], v[72:75]\n"
      "s_waitcnt lgkmcnt(4)\n"
      "v_mfma_f32_16x16x32_bf16 v[76:79], v[48:51], v[156:159], v[76:79]\n"
      "s_waitcnt lgkmcnt(2)\n"
      "s_waitcnt lgkmcnt(0)\n"
      "s_barrier\n"
      "v_mfma_f32_16x16x32_bf16 v[80:83], v[52:55], v[144:147], v[80:83]\n"
      "ds_read_b64_tr_b16 v[32:33], %[wa0] offset:24576\n"
      "ds_read_b64_tr_b16 v[34:35], %[wa0] offset:25088\n"
      "v_mfma_f32_16x16x32_bf16 v[84:87], v[52:55], v[148:151], v[84:87]\n"
      "ds_read_b128 v[128:131], %[xa0] offset:0\n"
      "ds_read_b128 v[132:135], %[xa0] offset:2048\n"
      "v_mfma_f32_16x16x32_bf16 v[88:91], v[52:55], v[152:155], v[88:91]\n"
      "ds_read_b128 v[136:139], %[xa0] offset:4096\n"
      "v_mfma_f32_16x16x32_bf16 v[92:95], v[52:55], v[156:159], v[92:95]\n"
      "ds_read_b128 v[140:143], %[xa0] offset:6144\n"
      "v_add_u32 %[xa0], 0x2000, %[xa0]\n"
      "v_mfma_f32_16x16x32_bf16 v[96:99], v[56:59], v[144:147], v[96:99]\n"
      "ds_read_b64_tr_b16 v[36:37], %[wa1] offset:24576\n"
      "v_mfma_f32_16x16x32_bf16 v[100:103], v[56:59], v[148:151], v[100:103]\n"
      "ds_read_b64_tr_b16 v[38:39], %[wa1] offset:25088\n"
      "v_mfma_f32_16x16x32_bf16 v[104:107], v[56:59], v[152:155], v[104:107]\n"
      "ds_read_b64_tr_b16 v[40:41], %[wa2] offset:24576\n"
      "v_mfma_f32_16x16x32_bf16 v[108:111], v[56:59], v[156:159], v[108:111]\n"
      "ds_read_b64_tr_b16 v[42:43], %[wa2] offset:25088\n"
      "s_waitcnt lgkmcnt(7)\n"
      "v_mfma_f32_16x16x32_bf16 v[64:67], v[32:35], v[128:131], v[64:67]\n"
      "ds_read_b64_tr_b16 v[48:49], %[wa0] offset:27648\n"
      "s_waitcnt lgkmcnt(7)\n"
      "v_mfma_f32_16x16x32_bf16 v[68:71], v[32:35], v[132:135], v[68:71]\n"
      "ds_read_b64_tr_b16 v[50:51], %[wa0] offset:28160\n"
      "s_waitcnt lgkmcnt(7)\n"
      "v_mfma_f32_16x16x32_bf16 v[72:75], v[32:35], v[136:139], v[72:75]\n"
      "ds_read_b128 v[144:147], %[xa1] offset:0\n"
      "s_waitcnt lgkmcnt(7)\n"
      "v_mfma_f32_16x16x32_bf16 v[76:79], v[32:35], v[140:143], v[76:79]\n"
      "ds_read_b128 v[148:151], %[xa1] offset:2048\n"
      "s_waitcnt lgkmcnt(6)\n"
      "v_mfma_f32_16x16x32_bf16 v[80:83], v[36:39], v[128:131], v[80:83]\n"
      "ds_read_b128 v[152:155], %[xa1] offset:4096\n"
      "v_mfma_f32_16x16x32_bf16 v[84:87], v[36:39], v[132:135], v[84:87]\n"
      "ds_read_b128 v[156:159], %[xa1] offset:6144\n"
      "v_add_u32 %[xa1], 0x2000, %[xa1]\n"
      "v_mfma_f32_16x16x32_bf16 v[88:91], v[36:39], v[136:139], v[88:91]\n"
      "ds_read_b64_tr_b16 v[52:53], %[wa1] offset:27648\n"
      "v_mfma_f32_16x16x32_bf16 v[92:95], v[36:39], v[140:143], v[92:95]\n"
      "ds_read_b64_tr_b16 v[54:55], %[wa1] offset:28160\n"
      "s_waitcnt lgkmcnt(8)\n"
      "v_mfma_f32_16x16x32_bf16 v[96:99], v[40:43], v[128:131], v[96:99]\n"
      "ds_read_b64_tr_b16 v[56:57], %[wa2] offset:27648\n"
      "v_mfma_f32_16x16x32_bf16 v[100:103], v[40:43], v[132:135], v[100:103]\n"
      "ds_read_b64_tr_b16 v[58:59], %[wa2] offset:28160\n"
      "v_mfma_f32_16x16x32_bf16 v[104:107], v[40:43], v[136:139], v[104:107]\n"
      "v_mfma_f32_16x16x32_bf16 v[108:111], v[40:43], v[140:143], v[108:111]\n"
      "s_waitcnt lgkmcnt(7)\n"
      "v_mfma_f32_16x16x32_bf16 v[64:67], v[48:51], v[144:147], v[64:67]\n"
      "s_waitcnt lgkmcnt(6)\n"
      "v_mfma_f32_16x16x32_bf16 v[68:71], v[48:51], v[148:151], v[68:71]\n"
      "s_waitcnt lgkmcnt(5)\n"
      "v_mfma_f32_16x16x32_bf16 v[72:75], v[48:51], v[152:155], v[72:75]\n"
      "s_waitcnt lgkmcnt(4)\n"
      "v_mfma_f32_16x16x32_bf16 v[76:79], v[48:51], v[156:159], v[76:79]\n"
      "s_waitcnt lgkmcnt(2)\n"
      "s_waitcnt lgkmcnt(0)\n"
      "s_barrier\n"
      "v_mfma_f32_16x16x32_bf16 v[80:83], v[52:55], v[144:147], v[80:83]\n"
      "ds_read_b64_tr_b16 v[32:33], %[wa0] offset:49152\n"
      "ds_read_b64_tr_b16 v[34:35], %[wa0] offset:49664\n"
      "v_mfma_f32_16x16x32_bf16 v[84:87], v[52:55], v[148:151], v[84:87]\n"
      "ds_read_b128 v[128:131], %[xa0] offset:0\n"
      "ds_read_b128 v[132:135], %[xa0] offset:2048\n"
      "v_mfma_f32_16x16x32_bf16 v[88:91], v[52:55], v[152:155], v[88:91]\n"
      "ds_read_b128 v[136:139], %[xa0] offset:4096\n"
      "v_mfma_f32_16x16x32_bf16 v[92:95], v[52:55], v[156:159], v[92:95]\n"
      "ds_read_b128 v[140:143], %[xa0] offset:6144\n"
      "v_add_u32 %[xa0], 0x2000, %[xa0]\n"
      "v_mfma_f32_16x16x32_bf16 v[96:99], v[56:59], v[144:147], v[96:99]\n"
      "ds_read_b64_tr_b16 v[36:37], %[wa1] offset:49152\n"
      "v_mfma_f32_16x16x32_bf16 v[100:103], v[56:59], v[148:151], v[100:103]\n"
      "ds_read_b64_tr_b16 v[38:39], %[wa1] offset:49664\n"
      "v_mfma_f32_16x16x32_bf16 v[104:107], v[56:59], v[152:155], v[104:107]\n"
      "ds_read_b64_tr_b16 v[40:41], %[wa2] offset:49152\n"
      "v_mfma_f32_16x16x32_bf16 v[108:111], v[56:59], v[156:159], v[108:111]\n"
      "ds_read_b64_tr_b16 v[42:43], %[wa2] offset:49664\n"
      "s_waitcnt lgkmcnt(7)\n"
      "v_mfma_f32_16x16x32_bf16 v[64:67], v[32:35], v[128:131], v[64:67]\n"
      "ds_read_b64_tr_b16 v[48:49], %[wa0] offset:52224\n"
      "s_waitcnt lgkmcnt(7)\n"
      "v_mfma_f32_16x16x32_bf16 v[68:71], v[32:35], v[132:135], v[68:71]\n"
      "ds_read_b64_tr_b16 v[50:51], %[wa0] offset:52736\n"
      "s_waitcnt lgkmcnt(7)\n"
      "v_mfma_f32_16x16x32_bf16 v[72:75], v[32:35], v[136:139], v[72:75]\n"
      "ds_read_b128 v[144:147], %[xa1] offset:0\n"
      "s_waitcnt lgkmcnt(7)\n"
      "v_mfma_f32_16x16x32_bf16 v[76:79], v[32:35], v[140:143], v[76:79]\n"
      "ds_read_b128 v[148:151], %[xa1] offset:2048\n"
      "s_waitcnt lgkmcnt(6)\n"
      "v_mfma_f32_16x16x32_bf16 v[80:83], v[36:39], v[128:131], v[80:83]\n"
      "ds_read_b128 v[152:155], %[xa1] offset:4096\n"
      "v_mfma_f32_16x16x32_bf16 v[84:87], v[36:39], v[132:135], v[84:87]\n"
      "ds_read_b128 v[156:159], %[xa1] offset:6144\n"
      "v_add_u32 %[xa1], 0x2000, %[xa1]\n"
      "v_mfma_f32_16x16x32_bf16 v[88:91], v[36:39], v[136:139], v[88:91]\n"
      "ds_read_b64_tr_b16 v[52:53], %[wa1] offset:52224\n"
      "v_mfma_f32_16x16x32_bf16 v[92:95], v[36:39], v[140:143], v[92:95]\n"
      "ds_read_b64_tr_b16 v[54:55], %[wa1] offset:52736\n"
      "s_waitcnt lgkmcnt(8)\n"
      "v_mfma_f32_16x16x32_bf16 v[96:99], v[40:43], v[128:131], v[96:99]\n"
      "ds_read_b64_tr_b16 v[56:57], %[wa2] offset:52224\n"
      "v_mfma_f32_16x16x32_bf16 v[100:103], v[40:43], v[132:135], v[100:103]\n"
      "ds_read_b64_tr_b16 v[58:59], %[wa2] offset:52736\n"
      "v_mfma_f32_16x16x32_bf16 v[104:107], v[40:43], v[136:139], v[104:107]\n"
      "v_mfma_f32_16x16x32_bf16 v[108:111], v[40:43], v[140:143], v[108:111]\n"
      "s_waitcnt lgkmcnt(7)\n"
      "v_mfma_f32_16x16x32_bf16 v[64:67], v[48:51], v[144:147], v[64:67]\n"
      "s_waitcnt lgkmcnt(6)\n"
      "v_mfma_f32_16x16x32_bf16 v[68:71], v[48:51], v[148:151], v[68:71]\n"
      "s_waitcnt lgkmcnt(5)\n"
      "v_mfma_f32_16x16x32_bf16 v[72:75], v[48:51], v[152:155], v[72:75]\n"
      "s_waitcnt lgkmcnt(4)\n"
      "v_mfma_f32_16x16x32_bf16 v[76:79], v[48:51], v[156:159], v[76:79]\n"
      "s_waitcnt lgkmcnt(2)\n"
      "s_waitcnt lgkmcnt(0)\n"
      "s_barrier\n"
      "v_mfma_f32_16x16x32_bf16 v[80:83], v[52:55], v[144:147], v[80:83]\n"
      "ds_read_b64_tr_b16 v[32:33], %[wa0] offset:0\n"
      "ds_read_b64_tr_b16 v[34:35], %[wa0] offset:512\n"
      "v_mfma_f32_16x16x32_bf16 v[84:87], v[52:55], v[148:151], v[84:87]\n"
      "ds_read_b128 v[128:131], %[xa0] offset:0\n"
      "ds_read_b128 v[132:135], %[xa0] offset:2048\n"
      "v_mfma_f32_16x16x32_bf16 v[88:91], v[52:55], v[152:155], v[88:91]\n"
      "ds_read_b128 v[136:139], %[xa0] offset:4096\n"
      "v_mfma_f32_16x16x32_bf16 v[92:95], v[52:55], v[156:159], v[92:95]\n"
      "ds_read_b128 v[140:143], %[xa0] offset:6144\n"
      "v_add_u32 %[xa0], 0x2000, %[xa0]\n"
      "v_mfma_f32_16x16x32_bf16 v[96:99], v[56:59], v[144:147], v[96:99]\n"
      "ds_read_b64_tr_b16 v[36:37], %[wa1] offset:0\n"
      "v_mfma_f32_16x16x32_bf16 v[100:103], v[56:59], v[148:151], v[100:103]\n"
      "ds_read_b64_tr_b16 v[38:39], %[wa1] offset:512\n"
      "v_mfma_f32_16x16x32_bf16 v[104:107], v[56:59], v[152:155], v[104:107]\n"
      "ds_read_b64_tr_b16 v[40:41], %[wa2] offset:0\n"
      "v_mfma_f32_16x16x32_bf16 v[108:111], v[56:59], v[156:159], v[108:111]\n"
      "ds_read_b64_tr_b16 v[42:43], %[wa2] offset:512\n"
      "s_waitcnt lgkmcnt(7)\n"
      "v_mfma_f32_16x16x32_bf16 v[64:67], v[32:35], v[128:131], v[64:67]\n"
      "ds_read_b64_tr_b16 v[48:49], %[wa0] offset:3072\n"
      "s_waitcnt lgkmcnt(7)\n"
      "v_mfma_f32_16x16x32_bf16 v[68:71], v[32:35], v[132:135], v[68:71]\n"
      "ds_read_b64_tr_b16 v[50:51], %[wa0] offset:3584\n"
      "s_waitcnt lgkmcnt(7)\n"
      "v_mfma_f32_16x16x32_bf16 v[72:75], v[32:35], v[136:139], v[72:75]\n"
      "ds_read_b128 v[144:147], %[xa1] offset:0\n"
      "s_waitcnt lgkmcnt(7)\n"
      "v_mfma_f32_16x16x32_bf16 v[76:79], v[32:35], v[140:143], v[76:79]\n"
      "ds_read_b128 v[148:151], %[xa1] offset:2048\n"
      "s_waitcnt lgkmcnt(6)\n"
      "v_mfma_f32_16x16x32_bf16 v[80:83], v[36:39], v[128:131], v[80:83]\n"
      "ds_read_b128 v[152:155], %[xa1] offset:4096\n"
      "v_mfma_f32_16x16x32_bf16 v[84:87], v[36:39], v[132:135], v[84:87]\n"
      "ds_read_b128 v[156:159], %[xa1] offset:6144\n"
      "v_add_u32 %[xa1], 0x2000, %[xa1]\n"
      "v_mfma_f32_16x16x32_bf16 v[88:91], v[36:39], v[136:139], v[88:91]\n"
      "ds_read_b64_tr_b16 v[52:53], %[wa1] offset:3072\n"
      "v_mfma_f32_16x16x32_bf16 v[92:95], v[36:39], v[140:143], v[92:95]\n"
      "ds_read_b64_tr_b16 v[54:55], %[wa1] offset:3584\n"
      "s_waitcnt lgkmcnt(8)\n"
      "v_mfma_f32_16x16x32_bf16 v[96:99], v[40:43], v[128:131], v[96:99]\n"
      "ds_read_b64_tr_b16 v[56:57], %[wa2] offset:3072\n"
      "v_mfma_f32_16x16x32_bf16 v[100:103], v[40:43], v[132:135], v[100:103]\n"
      "ds_read_b64_tr_b16 v[58:59], %[wa2] offset:3584\n"
      "v_mfma_f32_16x16x32_bf16 v[104:107], v[40:43], v[136:139], v[104:107]\n"
      "v_mfma_f32_16x16x32_bf16 v[108:111], v[40:43], v[140:143], v[108:111]\n"
      "s_waitcnt lgkmcnt(7)\n"
      "v_mfma_f32_16x16x32_bf16 v[64:67], v[48:51], v[144:147], v[64:67]\n"
      "s_waitcnt lgkmcnt(6)\n"
      "v_mfma_f32_16x16x32_bf16 v[68:71], v[48:51], v[148:151], v[68:71]\n"
      "s_waitcnt lgkmcnt(5)\n"
      "v_mfma_f32_16x16x32_bf16 v[72:75], v[48:51], v[152:155], v[72:75]\n"
      "s_waitcnt lgkmcnt(4)\n"
      "v_mfma_f32_16x16x32_bf16 v[76:79], v[48:51], v[156:159], v[76:79]\n"
      "s_waitcnt lgkmcnt(2)\n"
      "s_waitcnt lgkmcnt(0)\n"
      "s_barrier\n"
      "v_mfma_f32_16x16x32_bf16 v[80:83], v[52:55], v[144:147], v[80:83]\n"
      "ds_read_b64_tr_b16 v[32:33], %[wa0] offset:24576\n"
      "ds_read_b64_tr_b16 v[34:35], %[wa0] offset:25088\n"
      "v_mfma_f32_16x16x32_bf16 v[84:87], v[52:55], v[148:151], v[84:87]\n"
      "ds_read_b128 v[128:131], %[xa0] offset:0\n"
      "ds_read_b128 v[132:135], %[xa0] offset:2048\n"
      "v_mfma_f32_16x16x32_bf16 v[88:91], v[52:55], v[152:155], v[88:91]\n"
      "ds_read_b128 v[136:139], %[xa0] offset:4096\n"
      "v_mfma_f32_16x16x32_bf16 v[92:95], v[52:55], v[156:159], v[92:95]\n"
      "ds_read_b128 v[140:143], %[xa0] offset:6144\n"
      "v_add_u32 %[xa0], 0x2000, %[xa0]\n"
      "v_mfma_f32_16x16x32_bf16 v[96:99], v[56:59], v[144:147], v[96:99]\n"
      "ds_read_b64_tr_b16 v[36:37], %[wa1] offset:24576\n"
      "v_mfma_f32_16x16x32_bf16 v[100:103], v[56:59], v[148:151], v[100:103]\n"
      "ds_read_b64_tr_b16 v[38:39], %[wa1] offset:25088\n"
      "v_mfma_f32_16x16x32_bf16 v[104:107], v[56:59], v[152:155], v[104:107]\n"
      "ds_read_b64_tr_b16 v[40:41], %[wa2] offset:24576\n"
      "v_mfma_f32_16x16x32_bf16 v[108:111], v[56:59], v[156:159], v[108:111]\n"
      "ds_read_b64_tr_b16 v[42:43], %[wa2] offset:25088\n"
      "s_waitcnt lgkmcnt(7)\n"
      "v_mfma_f32_16x16x32_bf16 v[64:67], v[32:35], v[128:131], v[64:67]\n"
      "ds_read_b64_tr_b16 v[48:49], %[wa0] offset:27648\n"
      "s_waitcnt lgkmcnt(7)\n"
      "v_mfma_f32_16x16x32_bf16 v[68:71], v[32:35], v[132:135], v[68:71]\n"
      "ds_read_b64_tr_b16 v[50:51], %[wa0] offset:28160\n"
      "s_waitcnt lgkmcnt(7)\n"
      "v_mfma_f32_16x16x32_bf16 v[72:75], v[32:35], v[136:139], v[72:75]\n"
      "ds_read_b128 v[144:147], %[xa1] offset:0\n"
      "s_waitcnt lgkmcnt(7)\n"
      "v_mfma_f32_16x16x32_bf16 v[76:79], v[32:35], v[140:143], v[76:79]\n"
      "ds_read_b128 v[148:151], %[xa1] offset:2048\n"
      "s_waitcnt lgkmcnt(6)\n"
      "v_mfma_f32_16x16x32_bf16 v[80:83], v[36:39], v[128:131], v[80:83]\n"
      "ds_read_b128 v[152:155], %[xa1] offset:4096\n"
      "v_mfma_f32_16x16x32_bf16 v[84:87], v[36:39], v[132:135], v[84:87]\n"
      "ds_read_b128 v[156:159], %[xa1] offset:6144\n"
      "v_add_u32 %[xa1], 0x2000, %[xa1]\n"
      "v_mfma_f32_16x16x32_bf16 v[88:91], v[36:39], v[136:139], v[88:91]\n"
      "ds_read_b64_tr_b16 v[52:53], %[wa1] offset:27648\n"
      "v_mfma_f32_16x16x32_bf16 v[92:95], v[36:39], v[140:143], v[92:95]\n"
      "ds_read_b64_tr_b16 v[54:55], %[wa1] offset:28160\n"
      "s_waitcnt lgkmcnt(8)\n"
      "v_mfma_f32_16x16x32_bf16 v[96:99], v[40:43], v[128:131], v[96:99]\n"
      "ds_read_b64_tr_b16 v[56:57], %[wa2] offset:27648\n"
      "v_mfma_f32_16x16x32_bf16 v[100:103], v[40:43], v[132:135], v[100:103]\n"
      "ds_read_b64_tr_b16 v[58:59], %[wa2] offset:28160\n"
      "v_mfma_f32_16x16x32_bf16 v[104:107], v[40:43], v[136:139], v[104:107]\n"
      "v_mfma_f32_16x16x32_bf16 v[108:111], v[40:43], v[140:143], v[108:111]\n"
      "s_waitcnt lgkmcnt(7)\n"
      "v_mfma_f32_16x16x32_bf16 v[64:67], v[48:51], v[144:147], v[64:67]\n"
      "s_waitcnt lgkmcnt(6)\n"
      "v_mfma_f32_16x16x32_bf16 v[68:71], v[48:51], v[148:151], v[68:71]\n"
      "s_waitcnt lgkmcnt(5)\n"
      "v_mfma_f32_16x16x32_bf16 v[72:75], v[48:51], v[152:155], v[72:75]\n"
      "s_waitcnt lgkmcnt(4)\n"
      "v_mfma_f32_16x16x32_bf16 v[76:79], v[48:51], v[156:159], v[76:79]\n"
      "s_waitcnt lgkmcnt(2)\n"
      "v_mfma_f32_16x16x32_bf16 v[80:83], v[52:55], v[144:147], v[80:83]\n"
      "v_mfma_f32_16x16x32_bf16 v[84:87], v[52:55], v[148:151], v[84:87]\n"
      "v_mfma_f32_16x16x32_bf16 v[88:91], v[52:55], v[152:155], v[88:91]\n"
      "v_mfma_f32_16x16x32_bf16 v[92:95], v[52:55], v[156:159], v[92:95]\n"
      "s_waitcnt lgkmcnt(0)\n"
      "v_mfma_f32_16x16x32_bf16 v[96:99], v[56:59], v[144:147], v[96:99]\n"
      "v_mfma_f32_16x16x32_bf16 v[100:103], v[56:59], v[148:151], v[100:103]\n"
      "v_mfma_f32_16x16x32_bf16 v[104:107], v[56:59], v[152:155], v[104:107]\n"
      "v_mfma_f32_16x16x32_bf16 v[108:111], v[56:59], v[156:159], v[108:111]\n"
      "s_nop 7\n"
      : "=&{v[64:79]}"(acc[0]), "=&{v[80:95]}"(acc[1]), "=&{v[96:111]}"(acc[2]), [xa0] "+v"(xa0), [xa1] "+v"(xa1)
      : [wa0] "v"(wa[0]), [wa1] "v"(wa[1]), [wa2] "v"(wa[2]), [b0] "v"(bias[0]), [b1] "v"(bias[1]), [b2] "v"(bias[2])
      : "v32", "v33", "v34", "v35", "v36", "v37", "v38", "v39", "v40", "v41", "v42", "v43", "v44", "v45", "v46", "v47", "v48", "v49", "v50", "v51", "v52", "v53", "v54", "v55", "v56", "v57", "v58", "v59", "v60", "v61", "v62", "v63", "v128", "v129", "v130", "v131", "v132", "v133", "v134", "v135", "v136", "v137", "v138", "v139", "v140", "v141", "v142", "v143", "v144", "v145", "v146", "v147", "v148", "v149", "v150", "v151", "v152", "v153", "v154", "v155", "v156", "v157", "v158", "v159", "memory");
#pragma unroll
    for (int n = 0; n < 3; ++n) {
      f32x16 a;
#pragma unroll
      for (int i = 0; i < 16; ++i) { const float v = __int_as_float(max(__float_as_int(acc[n][i]), 0)); a[i] = v * v; }
#pragma unroll
      for (int p = 0; p < 2; ++p) {
        dv[2 * n + p] = pairimg(a, p); asm volatile("" : "+v"(dv[2 * n + p]));
        da[2 * n + p] = WS_H + ((4 * rb + 2 * half + p) * 96 + 12 * fb + 3 * wid + n) * 1024;
      }
    }
    ns = 6;
    bar();
  }
  }
  const int qt0 = qb * 8 + wid;
  const u32 q = sm + AT_Q + (wid >> 2) * AT_PITCH + (wid & 3) * Q_TILE;
  const bf16x8 qm = lds128(q + lane * 16);
  u32x2 t2 = *(LDSP(u32x2))(q + 1024 + l32 * 8);
  const float gq = *(LDSP(float))(sm + AT_Q + 2 * AT_PITCH + (wid * 32 + l32) * 4);
  const s16x4 qt = __builtin_bit_cast(s16x4, u32x2{hf ? 0u : t2[0], hf ? 0u : t2[1]});
  const int vrow = l32 < 28 ? l32 : l32 - 16;
  const bool ones = vrow >= 20;
  const u32 ko = 1280 + lane * 16, xo = 1280 + 1024 + l32 * 8;
  u32 vo[2], va[2][2];
#pragma unroll
  for (int s = 0; s < 2; ++s) {
    const u32 c = ((2 * s + hf) ^ (ones ? 1 : (vrow >> 2) & 3)) << 4;
    vo[s] = (ones ? 0 : vrow * 64) + c;
    const u32 pad = l32 > 20 ? ((2 * s + hf) ^ (l32 < 28 ? 1 : 3)) << 4 : 64 + c;
    const bool inpad = l32 >= 20;
    va[0][s] = sm + (inpad ? pad : AT_PAD + vo[s]);
    va[1][s] = sm + AT_PAD + (inpad ? KV_PAIR + pad : 3840 + vo[s]);
  }
  f32x16 o = {};
  {
    const f32x16 zero = {};
    f32x16 s = qk(lds128(sm + AT_PAD + ko), *(LDSP(s16x4))(sm + AT_PAD + xo), qm, qt, zero);
    const float m = halfmax(max16(s));
    f32x16 cm;
#pragma unroll
    for (int i = 0; i < 16; ++i) { s[i] -= m; cm[i] = -m; }
    u32 it;
#pragma unroll
    for (int i = 0; i < 6; ++i) da[i] = rfl(da[i] - (sm + AT_PAD + 1280));
    ns = rfl(ns);
    asm volatile(
      "ds_read_b128 v[112:115], %[ka] offset:1280\n"
      "ds_read_b64 v[124:125], %[kx1] offset:1280\n"
      "ds_read_b128 v[116:119], %[va00] offset:0\n"
      "s_mov_b32 %[it], 2\n"
      "s_waitcnt lgkmcnt(1)\n"
      "v_mfma_f32_32x32x16_bf16 v[80:95], v[112:115], %[qm], %[cm]\n"
      "ds_read_b128 v[112:115], %[ka] offset:5248\n"
      "v_exp_f32 v64, v64\n"
      "v_exp_f32 v65, v65\n"
      "v_exp_f32 v66, v66\n"
      "v_exp_f32 v67, v67\n"
      "s_cmp_gt_u32 %[ns], 0\n"
      "s_cbranch_scc0 2f\n"
      "buffer_store_dwordx4 %[d0], %[ka], %[rs], %[a0] offen sc1\n"
      "2:\n"
      "v_mfma_f32_32x32x8_bf16 v[80:95], v[124:125], %[qt], v[80:95]\n"
      "ds_read_b64 v[124:125], %[kx0] offset:5248\n"
      "v_exp_f32 v68, v68\n"
      "v_exp_f32 v69, v69\n"
      "v_exp_f32 v70, v70\n"
      "v_exp_f32 v71, v71\n"
      "s_waitcnt lgkmcnt(2)\n"
      "ds_read_b128 v[120:123], %[va01] offset:0\n"
      "v_exp_f32 v72, v72\n"
      "v_exp_f32 v73, v73\n"
      "v_exp_f32 v74, v74\n"
      "v_exp_f32 v75, v75\n"
      "s_cmp_gt_u32 %[ns], 1\n"
      "s_cbranch_scc0 2f\n"
      "buffer_store_dwordx4 %[d1], %[ka], %[rs], %[a1] offen sc1\n"
      "2:\n"
      "v_smfmac_f32_32x32x32_bf16 %[o], v[116:119], v[64:71], %[ix]\n"
      "ds_read_b128 v[116:119], %[va10] offset:0\n"
      "v_exp_f32 v76, v76\n"
      "v_exp_f32 v77, v77\n"
      "v_exp_f32 v78, v78\n"
      "v_exp_f32 v79, v79\n"
      "s_waitcnt lgkmcnt(2)\n"
      "v_mfma_f32_32x32x16_bf16 v[96:111], v[112:115], %[qm], %[cm]\n"
      "ds_read_b128 v[112:115], %[ka] offset:6528\n"
      "v_exp_f32 v80, v80\n"
      "v_exp_f32 v81, v81\n"
      "v_exp_f32 v82, v82\n"
      "v_exp_f32 v83, v83\n"
      "s_cmp_gt_u32 %[ns], 2\n"
      "s_cbranch_scc0 2f\n"
      "buffer_store_dwordx4 %[d2], %[ka], %[rs], %[a2] offen sc1\n"
      "2:\n"
      "v_mfma_f32_32x32x8_bf16 v[96:111], v[124:125], %[qt], v[96:111]\n"
      "ds_read_b64 v[124:125], %[kx1] offset:6528\n"
      "v_exp_f32 v84, v84\n"
      "v_exp_f32 v85, v85\n"
      "v_exp_f32 v86, v86\n"
      "v_exp_f32 v87, v87\n"
      "s_waitcnt lgkmcnt(2)\n"
      "v_smfmac_f32_32x32x32_bf16 %[o], v[120:123], v[72:79], %[ix]\n"
      "ds_read_b128 v[120:123], %[va11] offset:0\n"
      "v_exp_f32 v88, v88\n"
      "v_exp_f32 v89, v89\n"
      "v_exp_f32 v90, v90\n"
      "v_exp_f32 v91, v91\n"
      "s_cmp_gt_u32 %[ns], 3\n"
      "s_cbranch_scc0 2f\n"
      "buffer_store_dwordx4 %[d3], %[ka], %[rs], %[a3] offen sc1\n"
      "2:\n"
      "v_smfmac_f32_32x32x32_bf16 %[o], v[116:119], v[80:87], %[ix]\n"
      "ds_read_b128 v[116:119], %[va00] offset:5248\n"
      "v_exp_f32 v92, v92\n"
      "v_exp_f32 v93, v93\n"
      "v_exp_f32 v94, v94\n"
      "v_exp_f32 v95, v95\n"
      "s_waitcnt lgkmcnt(2)\n"
      "v_mfma_f32_32x32x16_bf16 v[64:79], v[112:115], %[qm], %[cm]\n"
      "ds_read_b128 v[112:115], %[ka] offset:10496\n"
      "v_exp_f32 v96, v96\n"
      "v_exp_f32 v97, v97\n"
      "v_exp_f32 v98, v98\n"
      "v_exp_f32 v99, v99\n"
      "s_cmp_gt_u32 %[ns], 4\n"
      "s_cbranch_scc0 2f\n"
      "buffer_store_dwordx4 %[d4], %[ka], %[rs], %[a4] offen sc1\n"
      "2:\n"
      "v_mfma_f32_32x32x8_bf16 v[64:79], v[124:125], %[qt], v[64:79]\n"
      "ds_read_b64 v[124:125], %[kx0] offset:10496\n"
      "v_exp_f32 v100, v100\n"
      "v_exp_f32 v101, v101\n"
      "v_exp_f32 v102, v102\n"
      "v_exp_f32 v103, v103\n"
      "s_waitcnt lgkmcnt(2)\n"
      "v_smfmac_f32_32x32x32_bf16 %[o], v[120:123], v[88:95], %[ix]\n"
      "ds_read_b128 v[120:123], %[va01] offset:5248\n"
      "v_exp_f32 v104, v104\n"
      "v_exp_f32 v105, v105\n"
      "v_exp_f32 v106, v106\n"
      "v_exp_f32 v107, v107\n"
      "s_cmp_gt_u32 %[ns], 5\n"
      "s_cbranch_scc0 2f\n"
      "buffer_store_dwordx4 %[d5], %[ka], %[rs], %[a5] offen sc1\n"
      "2:\n"
      "v_smfmac_f32_32x32x32_bf16 %[o], v[116:119], v[96:103], %[ix]\n"
      "ds_read_b128 v[116:119], %[va10] offset:5248\n"
      "v_exp_f32 v108, v108\n"
      "v_exp_f32 v109, v109\n"
      "v_exp_f32 v110, v110\n"
      "v_exp_f32 v111, v111\n"
      "s_waitcnt lgkmcnt(2)\n"
      "v_mfma_f32_32x32x16_bf16 v[80:95], v[112:115], %[qm], %[cm]\n"
      "ds_read_b128 v[112:115], %[ka] offset:11776\n"
      "v_exp_f32 v64, v64\n"
      "v_exp_f32 v65, v65\n"
      "v_exp_f32 v66, v66\n"
      "v_exp_f32 v67, v67\n"
      "v_mfma_f32_32x32x8_bf16 v[80:95], v[124:125], %[qt], v[80:95]\n"
      "ds_read_b64 v[124:125], %[kx1] offset:11776\n"
      "v_exp_f32 v68, v68\n"
      "v_exp_f32 v69, v69\n"
      "v_exp_f32 v70, v70\n"
      "v_exp_f32 v71, v71\n"
      "s_waitcnt lgkmcnt(2)\n"
      "v_smfmac_f32_32x32x32_bf16 %[o], v[120:123], v[104:111], %[ix]\n"
      "ds_read_b128 v[120:123], %[va11] offset:5248\n"
      "v_exp_f32 v72, v72\n"
      "v_exp_f32 v73, v73\n"
      "v_exp_f32 v74, v74\n"
      "v_exp_f32 v75, v75\n"
      "v_smfmac_f32_32x32x32_bf16 %[o], v[116:119], v[64:71], %[ix]\n"
      "ds_read_b128 v[116:119], %[va00] offset:10496\n"
      "v_exp_f32 v76, v76\n"
      "v_exp_f32 v77, v77\n"
      "v_exp_f32 v78, v78\n"
      "v_exp_f32 v79, v79\n"
      "s_waitcnt lgkmcnt(2)\n"
      "v_mfma_f32_32x32x16_bf16 v[96:111], v[112:115], %[qm], %[cm]\n"
      "ds_read_b128 v[112:115], %[ka] offset:15744\n"
      "v_exp_f32 v80, v80\n"
      "v_exp_f32 v81, v81\n"
      "v_exp_f32 v82, v82\n"
      "v_exp_f32 v83, v83\n"
      "v_mfma_f32_32x32x8_bf16 v[96:111], v[124:125], %[qt], v[96:111]\n"
      "ds_read_b64 v[124:125], %[kx0] offset:15744\n"
      "v_exp_f32 v84, v84\n"
      "v_exp_f32 v85, v85\n"
      "v_exp_f32 v86, v86\n"
      "v_exp_f32 v87, v87\n"
      "s_waitcnt lgkmcnt(2)\n"
      "v_smfmac_f32_32x32x32_bf16 %[o], v[120:123], v[72:79], %[ix]\n"
      "ds_read_b128 v[120:123], %[va01] offset:10496\n"
      "v_exp_f32 v88, v88\n"
      "v_exp_f32 v89, v89\n"
      "v_exp_f32 v90, v90\n"
      "v_exp_f32 v91, v91\n"
      "v_smfmac_f32_32x32x32_bf16 %[o], v[116:119], v[80:87], %[ix]\n"
      "ds_read_b128 v[116:119], %[va10] offset:10496\n"
      "v_exp_f32 v92, v92\n"
      "v_exp_f32 v93, v93\n"
      "v_exp_f32 v94, v94\n"
      "v_exp_f32 v95, v95\n"
      "s_waitcnt lgkmcnt(2)\n"
      "v_mfma_f32_32x32x16_bf16 v[64:79], v[112:115], %[qm], %[cm]\n"
      "ds_read_b128 v[112:115], %[ka] offset:17024\n"
      "v_exp_f32 v96, v96\n"
      "v_exp_f32 v97, v97\n"
      "v_exp_f32 v98, v98\n"
      "v_exp_f32 v99, v99\n"
      "v_mfma_f32_32x32x8_bf16 v[64:79], v[124:125], %[qt], v[64:79]\n"
      "ds_read_b64 v[124:125], %[kx1] offset:17024\n"
      "v_exp_f32 v100, v100\n"
      "v_exp_f32 v101, v101\n"
      "v_exp_f32 v102, v102\n"
      "v_exp_f32 v103, v103\n"
      "s_waitcnt lgkmcnt(2)\n"
      "v_smfmac_f32_32x32x32_bf16 %[o], v[120:123], v[88:95], %[ix]\n"
      "ds_read_b128 v[120:123], %[va11] offset:10496\n"
      "v_exp_f32 v104, v104\n"
      "v_exp_f32 v105, v105\n"
      "v_exp_f32 v106, v106\n"
      "v_exp_f32 v107, v107\n"
      "v_smfmac_f32_32x32x32_bf16 %[o], v[116:119], v[96:103], %[ix]\n"
      "ds_read_b128 v[116:119], %[va00] offset:15744\n"
      "v_exp_f32 v108, v108\n"
      "v_exp_f32 v109, v109\n"
      "v_exp_f32 v110, v110\n"
      "v_exp_f32 v111, v111\n"
      "s_waitcnt lgkmcnt(2)\n"
      "v_mfma_f32_32x32x16_bf16 v[80:95], v[112:115], %[qm], %[cm]\n"
      "s_waitcnt vmcnt(0)\n"
      "s_barrier\n"
      "ds_read_b128 v[112:115], %[ka] offset:20992\n"
      "v_exp_f32 v64, v64\n"
      "v_exp_f32 v65, v65\n"
      "v_exp_f32 v66, v66\n"
      "v_exp_f32 v67, v67\n"
      "v_mfma_f32_32x32x8_bf16 v[80:95], v[124:125], %[qt], v[80:95]\n"
      "ds_read_b64 v[124:125], %[kx0] offset:20992\n"
      "v_exp_f32 v68, v68\n"
      "v_exp_f32 v69, v69\n"
      "v_exp_f32 v70, v70\n"
      "v_exp_f32 v71, v71\n"
      "s_waitcnt lgkmcnt(2)\n"
      "v_smfmac_f32_32x32x32_bf16 %[o], v[120:123], v[104:111], %[ix]\n"
      "ds_read_b128 v[120:123], %[va01] offset:15744\n"
      "v_exp_f32 v72, v72\n"
      "v_exp_f32 v73, v73\n"
      "v_exp_f32 v74, v74\n"
      "v_exp_f32 v75, v75\n"
      "v_smfmac_f32_32x32x32_bf16 %[o], v[116:119], v[64:71], %[ix]\n"
      "ds_read_b128 v[116:119], %[va10] offset:15744\n"
      "v_exp_f32 v76, v76\n"
      "v_exp_f32 v77, v77\n"
      "v_exp_f32 v78, v78\n"
      "v_exp_f32 v79, v79\n"
      "s_waitcnt lgkmcnt(2)\n"
      "v_mfma_f32_32x32x16_bf16 v[96:111], v[112:115], %[qm], %[cm]\n"
      "ds_read_b128 v[112:115], %[ka] offset:22272\n"
      "v_exp_f32 v80, v80\n"
      "v_exp_f32 v81, v81\n"
      "v_exp_f32 v82, v82\n"
      "v_exp_f32 v83, v83\n"
      "v_mfma_f32_32x32x8_bf16 v[96:111], v[124:125], %[qt], v[96:111]\n"
      "ds_read_b64 v[124:125], %[kx1] offset:22272\n"
      "v_exp_f32 v84, v84\n"
      "v_exp_f32 v85, v85\n"
      "v_exp_f32 v86, v86\n"
      "v_exp_f32 v87, v87\n"
      "s_waitcnt lgkmcnt(2)\n"
      "v_smfmac_f32_32x32x32_bf16 %[o], v[120:123], v[72:79], %[ix]\n"
      "ds_read_b128 v[120:123], %[va11] offset:15744\n"
      "v_exp_f32 v88, v88\n"
      "v_exp_f32 v89, v89\n"
      "v_exp_f32 v90, v90\n"
      "v_exp_f32 v91, v91\n"
      "v_smfmac_f32_32x32x32_bf16 %[o], v[116:119], v[80:87], %[ix]\n"
      "ds_read_b128 v[116:119], %[va00] offset:20992\n"
      "v_exp_f32 v92, v92\n"
      "v_exp_f32 v93, v93\n"
      "v_exp_f32 v94, v94\n"
      "v_exp_f32 v95, v95\n"
      "s_waitcnt lgkmcnt(2)\n"
      "v_mfma_f32_32x32x16_bf16 v[64:79], v[112:115], %[qm], %[cm]\n"
      "ds_read_b128 v[112:115], %[ka] offset:26240\n"
      "v_exp_f32 v96, v96\n"
      "v_exp_f32 v97, v97\n"
      "v_exp_f32 v98, v98\n"
      "v_exp_f32 v99, v99\n"
      "v_mfma_f32_32x32x8_bf16 v[64:79], v[124:125], %[qt], v[64:79]\n"
      "ds_read_b64 v[124:125], %[kx0] offset:26240\n"
      "v_exp_f32 v100, v100\n"
      "v_exp_f32 v101, v101\n"
      "v_exp_f32 v102, v102\n"
      "v_exp_f32 v103, v103\n"
      "s_waitcnt lgkmcnt(2)\n"
      "v_smfmac_f32_32x32x32_bf16 %[o], v[120:123], v[88:95], %[ix]\n"
      "ds_read_b128 v[120:123], %[va01] offset:20992\n"
      "v_exp_f32 v104, v104\n"
      "v_exp_f32 v105, v105\n"
      "v_exp_f32 v106, v106\n"
      "v_exp_f32 v107, v107\n"
      "v_smfmac_f32_32x32x32_bf16 %[o], v[116:119], v[96:103], %[ix]\n"
      "ds_read_b128 v[116:119], %[va10] offset:20992\n"
      "v_exp_f32 v108, v108\n"
      "v_exp_f32 v109, v109\n"
      "v_exp_f32 v110, v110\n"
      "v_exp_f32 v111, v111\n"
      "s_waitcnt lgkmcnt(2)\n"
      "v_mfma_f32_32x32x16_bf16 v[80:95], v[112:115], %[qm], %[cm]\n"
      "ds_read_b128 v[112:115], %[ka] offset:27520\n"
      "v_exp_f32 v64, v64\n"
      "v_exp_f32 v65, v65\n"
      "v_exp_f32 v66, v66\n"
      "v_exp_f32 v67, v67\n"
      "v_mfma_f32_32x32x8_bf16 v[80:95], v[124:125], %[qt], v[80:95]\n"
      "ds_read_b64 v[124:125], %[kx1] offset:27520\n"
      "v_exp_f32 v68, v68\n"
      "v_exp_f32 v69, v69\n"
      "v_exp_f32 v70, v70\n"
      "v_exp_f32 v71, v71\n"
      "s_waitcnt lgkmcnt(2)\n"
      "v_smfmac_f32_32x32x32_bf16 %[o], v[120:123], v[104:111], %[ix]\n"
      "ds_read_b128 v[120:123], %[va11] offset:20992\n"
      "v_exp_f32 v72, v72\n"
      "v_exp_f32 v73, v73\n"
      "v_exp_f32 v74, v74\n"
      "v_exp_f32 v75, v75\n"
      "v_smfmac_f32_32x32x32_bf16 %[o], v[116:119], v[64:71], %[ix]\n"
      "ds_read_b128 v[116:119], %[va00] offset:26240\n"
      "v_exp_f32 v76, v76\n"
      "v_exp_f32 v77, v77\n"
      "v_exp_f32 v78, v78\n"
      "v_exp_f32 v79, v79\n"
      "s_waitcnt lgkmcnt(2)\n"
      "v_mfma_f32_32x32x16_bf16 v[96:111], v[112:115], %[qm], %[cm]\n"
      "ds_read_b128 v[112:115], %[ka] offset:31488\n"
      "v_exp_f32 v80, v80\n"
      "v_exp_f32 v81, v81\n"
      "v_exp_f32 v82, v82\n"
      "v_exp_f32 v83, v83\n"
      "v_mfma_f32_32x32x8_bf16 v[96:111], v[124:125], %[qt], v[96:111]\n"
      "ds_read_b64 v[124:125], %[kx0] offset:31488\n"
      "v_exp_f32 v84, v84\n"
      "v_exp_f32 v85, v85\n"
      "v_exp_f32 v86, v86\n"
      "v_exp_f32 v87, v87\n"
      "s_waitcnt lgkmcnt(2)\n"
      "v_smfmac_f32_32x32x32_bf16 %[o], v[120:123], v[72:79], %[ix]\n"
      "ds_read_b128 v[120:123], %[va01] offset:26240\n"
      "v_exp_f32 v88, v88\n"
      "v_exp_f32 v89, v89\n"
      "v_exp_f32 v90, v90\n"
      "v_exp_f32 v91, v91\n"
      "v_smfmac_f32_32x32x32_bf16 %[o], v[116:119], v[80:87], %[ix]\n"
      "ds_read_b128 v[116:119], %[va10] offset:26240\n"
      "v_exp_f32 v92, v92\n"
      "v_exp_f32 v93, v93\n"
      "v_exp_f32 v94, v94\n"
      "v_exp_f32 v95, v95\n"
      "s_waitcnt lgkmcnt(2)\n"
      "v_mfma_f32_32x32x16_bf16 v[64:79], v[112:115], %[qm], %[cm]\n"
      "ds_read_b128 v[112:115], %[ka] offset:32768\n"
      "v_exp_f32 v96, v96\n"
      "v_exp_f32 v97, v97\n"
      "v_exp_f32 v98, v98\n"
      "v_exp_f32 v99, v99\n"
      "v_mfma_f32_32x32x8_bf16 v[64:79], v[124:125], %[qt], v[64:79]\n"
      "ds_read_b64 v[124:125], %[kx1] offset:32768\n"
      "v_exp_f32 v100, v100\n"
      "v_exp_f32 v101, v101\n"
      "v_exp_f32 v102, v102\n"
      "v_exp_f32 v103, v103\n"
      "s_waitcnt lgkmcnt(2)\n"
      "v_smfmac_f32_32x32x32_bf16 %[o], v[120:123], v[88:95], %[ix]\n"
      "ds_read_b128 v[120:123], %[va11] offset:26240\n"
      "v_exp_f32 v104, v104\n"
      "v_exp_f32 v105, v105\n"
      "v_exp_f32 v106, v106\n"
      "v_exp_f32 v107, v107\n"
      "v_smfmac_f32_32x32x32_bf16 %[o], v[116:119], v[96:103], %[ix]\n"
      "ds_read_b128 v[116:119], %[va00] offset:31488\n"
      "v_exp_f32 v108, v108\n"
      "v_exp_f32 v109, v109\n"
      "v_exp_f32 v110, v110\n"
      "v_exp_f32 v111, v111\n"
      "s_waitcnt lgkmcnt(2)\n"
      "v_mfma_f32_32x32x16_bf16 v[80:95], v[112:115], %[qm], %[cm]\n"
      "ds_read_b128 v[112:115], %[ka] offset:36736\n"
      "v_exp_f32 v64, v64\n"
      "v_exp_f32 v65, v65\n"
      "v_exp_f32 v66, v66\n"
      "v_exp_f32 v67, v67\n"
      "v_mfma_f32_32x32x8_bf16 v[80:95], v[124:125], %[qt], v[80:95]\n"
      "ds_read_b64 v[124:125], %[kx0] offset:36736\n"
      "v_exp_f32 v68, v68\n"
      "v_exp_f32 v69, v69\n"
      "v_exp_f32 v70, v70\n"
      "v_exp_f32 v71, v71\n"
      "s_waitcnt lgkmcnt(2)\n"
      "v_smfmac_f32_32x32x32_bf16 %[o], v[120:123], v[104:111], %[ix]\n"
      "ds_read_b128 v[120:123], %[va01] offset:31488\n"
      "v_exp_f32 v72, v72\n"
      "v_exp_f32 v73, v73\n"
      "v_exp_f32 v74, v74\n"
      "v_exp_f32 v75, v75\n"
      "v_smfmac_f32_32x32x32_bf16 %[o], v[116:119], v[64:71], %[ix]\n"
      "ds_read_b128 v[116:119], %[va10] offset:31488\n"
      "v_exp_f32 v76, v76\n"
      "v_exp_f32 v77, v77\n"
      "v_exp_f32 v78, v78\n"
      "v_exp_f32 v79, v79\n"
      "s_waitcnt lgkmcnt(2)\n"
      "v_mfma_f32_32x32x16_bf16 v[96:111], v[112:115], %[qm], %[cm]\n"
      "ds_read_b128 v[112:115], %[ka] offset:38016\n"
      "v_exp_f32 v80, v80\n"
      "v_exp_f32 v81, v81\n"
      "v_exp_f32 v82, v82\n"
      "v_exp_f32 v83, v83\n"
      "v_mfma_f32_32x32x8_bf16 v[96:111], v[124:125], %[qt], v[96:111]\n"
      "ds_read_b64 v[124:125], %[kx1] offset:38016\n"
      "v_exp_f32 v84, v84\n"
      "v_exp_f32 v85, v85\n"
      "v_exp_f32 v86, v86\n"
      "v_exp_f32 v87, v87\n"
      "s_waitcnt lgkmcnt(2)\n"
      "v_smfmac_f32_32x32x32_bf16 %[o], v[120:123], v[72:79], %[ix]\n"
      "ds_read_b128 v[120:123], %[va11] offset:31488\n"
      "v_exp_f32 v88, v88\n"
      "v_exp_f32 v89, v89\n"
      "v_exp_f32 v90, v90\n"
      "v_exp_f32 v91, v91\n"
      "v_smfmac_f32_32x32x32_bf16 %[o], v[116:119], v[80:87], %[ix]\n"
      "ds_read_b128 v[116:119], %[va00] offset:36736\n"
      "v_exp_f32 v92, v92\n"
      "v_exp_f32 v93, v93\n"
      "v_exp_f32 v94, v94\n"
      "v_exp_f32 v95, v95\n"
      "s_waitcnt lgkmcnt(2)\n"
      "v_mfma_f32_32x32x16_bf16 v[64:79], v[112:115], %[qm], %[cm]\n"
      "s_barrier\n"
      "ds_read_b128 v[112:115], %[ka] offset:41984\n"
      "v_exp_f32 v96, v96\n"
      "v_exp_f32 v97, v97\n"
      "v_exp_f32 v98, v98\n"
      "v_exp_f32 v99, v99\n"
      "v_mfma_f32_32x32x8_bf16 v[64:79], v[124:125], %[qt], v[64:79]\n"
      "ds_read_b64 v[124:125], %[kx0] offset:41984\n"
      "v_exp_f32 v100, v100\n"
      "v_exp_f32 v101, v101\n"
      "v_exp_f32 v102, v102\n"
      "v_exp_f32 v103, v103\n"
      "s_waitcnt lgkmcnt(2)\n"
      "v_smfmac_f32_32x32x32_bf16 %[o], v[120:123], v[88:95], %[ix]\n"
      "ds_read_b128 v[120:123], %[va01] offset:36736\n"
      "v_exp_f32 v104, v104\n"
      "v_exp_f32 v105, v105\n"
      "v_exp_f32 v106, v106\n"
      "v_exp_f32 v107, v107\n"
      "v_smfmac_f32_32x32x32_bf16 %[o], v[116:119], v[96:103], %[ix]\n"
      "ds_read_b128 v[116:119], %[va10] offset:36736\n"
      "v_exp_f32 v108, v108\n"
      "v_exp_f32 v109, v109\n"
      "v_exp_f32 v110, v110\n"
      "v_exp_f32 v111, v111\n"
      "s_waitcnt lgkmcnt(2)\n"
      "v_mfma_f32_32x32x16_bf16 v[80:95], v[112:115], %[qm], %[cm]\n"
      "ds_read_b128 v[112:115], %[ka] offset:43264\n"
      "v_exp_f32 v64, v64\n"
      "v_exp_f32 v65, v65\n"
      "v_exp_f32 v66, v66\n"
      "v_exp_f32 v67, v67\n"
      "v_mfma_f32_32x32x8_bf16 v[80:95], v[124:125], %[qt], v[80:95]\n"
      "ds_read_b64 v[124:125], %[kx1] offset:43264\n"
      "v_exp_f32 v68, v68\n"
      "v_exp_f32 v69, v69\n"
      "v_exp_f32 v70, v70\n"
      "v_exp_f32 v71, v71\n"
      "s_waitcnt lgkmcnt(2)\n"
      "v_smfmac_f32_32x32x32_bf16 %[o], v[120:123], v[104:111], %[ix]\n"
      "ds_read_b128 v[120:123], %[va11] offset:36736\n"
      "v_exp_f32 v72, v72\n"
      "v_exp_f32 v73, v73\n"
      "v_exp_f32 v74, v74\n"
      "v_exp_f32 v75, v75\n"
      "v_smfmac_f32_32x32x32_bf16 %[o], v[116:119], v[64:71], %[ix]\n"
      "ds_read_b128 v[116:119], %[va00] offset:41984\n"
      "v_exp_f32 v76, v76\n"
      "v_exp_f32 v77, v77\n"
      "v_exp_f32 v78, v78\n"
      "v_exp_f32 v79, v79\n"
      "1:\n"
      "s_waitcnt lgkmcnt(2)\n"
      "v_mfma_f32_32x32x16_bf16 v[96:111], v[112:115], %[qm], %[cm]\n"
      "ds_read_b128 v[112:115], %[ka] offset:47232\n"
      "v_exp_f32 v80, v80\n"
      "v_exp_f32 v81, v81\n"
      "v_exp_f32 v82, v82\n"
      "v_exp_f32 v83, v83\n"
      "v_mfma_f32_32x32x8_bf16 v[96:111], v[124:125], %[qt], v[96:111]\n"
      "ds_read_b64 v[124:125], %[kx0] offset:47232\n"
      "v_exp_f32 v84, v84\n"
      "v_exp_f32 v85, v85\n"
      "v_exp_f32 v86, v86\n"
      "v_exp_f32 v87, v87\n"
      "s_waitcnt lgkmcnt(2)\n"
      "v_smfmac_f32_32x32x32_bf16 %[o], v[120:123], v[72:79], %[ix]\n"
      "ds_read_b128 v[120:123], %[va01] offset:41984\n"
      "v_exp_f32 v88, v88\n"
      "v_exp_f32 v89, v89\n"
      "v_exp_f32 v90, v90\n"
      "v_exp_f32 v91, v91\n"
      "v_smfmac_f32_32x32x32_bf16 %[o], v[116:119], v[80:87], %[ix]\n"
      "ds_read_b128 v[116:119], %[va10] offset:41984\n"
      "v_exp_f32 v92, v92\n"
      "v_exp_f32 v93, v93\n"
      "v_exp_f32 v94, v94\n"
      "v_exp_f32 v95, v95\n"
      "s_waitcnt lgkmcnt(2)\n"
      "v_mfma_f32_32x32x16_bf16 v[64:79], v[112:115], %[qm], %[cm]\n"
      "ds_read_b128 v[112:115], %[ka] offset:48512\n"
      "v_exp_f32 v96, v96\n"
      "v_exp_f32 v97, v97\n"
      "v_exp_f32 v98, v98\n"
      "v_exp_f32 v99, v99\n"
      "v_mfma_f32_32x32x8_bf16 v[64:79], v[124:125], %[qt], v[64:79]\n"
      "ds_read_b64 v[124:125], %[kx1] offset:48512\n"
      "v_exp_f32 v100, v100\n"
      "v_exp_f32 v101, v101\n"
      "v_exp_f32 v102, v102\n"
      "v_exp_f32 v103, v103\n"
      "s_waitcnt lgkmcnt(2)\n"
      "v_smfmac_f32_32x32x32_bf16 %[o], v[120:123], v[88:95], %[ix]\n"
      "ds_read_b128 v[120:123], %[va11] offset:41984\n"
      "v_exp_f32 v104, v104\n"
      "v_exp_f32 v105, v105\n"
      "v_exp_f32 v106, v106\n"
      "v_exp_f32 v107, v107\n"
      "v_smfmac_f32_32x32x32_bf16 %[o], v[116:119], v[96:103], %[ix]\n"
      "ds_read_b128 v[116:119], %[va00] offset:47232\n"
      "v_exp_f32 v108, v108\n"
      "v_exp_f32 v109, v109\n"
      "v_exp_f32 v110, v110\n"
      "v_exp_f32 v111, v111\n"
      "s_waitcnt lgkmcnt(2)\n"
      "v_mfma_f32_32x32x16_bf16 v[80:95], v[112:115], %[qm], %[cm]\n"
      "ds_read_b128 v[112:115], %[ka] offset:52480\n"
      "v_exp_f32 v64, v64\n"
      "v_exp_f32 v65, v65\n"
      "v_exp_f32 v66, v66\n"
      "v_exp_f32 v67, v67\n"
      "v_mfma_f32_32x32x8_bf16 v[80:95], v[124:125], %[qt], v[80:95]\n"
      "ds_read_b64 v[124:125], %[kx0] offset:52480\n"
      "v_exp_f32 v68, v68\n"
      "v_exp_f32 v69, v69\n"
      "v_exp_f32 v70, v70\n"
      "v_exp_f32 v71, v71\n"
      "s_waitcnt lgkmcnt(2)\n"
      "v_smfmac_f32_32x32x32_bf16 %[o], v[120:123], v[104:111], %[ix]\n"
      "ds_read_b128 v[120:123], %[va01] offset:47232\n"
      "v_exp_f32 v72, v72\n"
      "v_exp_f32 v73, v73\n"
      "v_exp_f32 v74, v74\n"
      "v_exp_f32 v75, v75\n"
      "v_smfmac_f32_32x32x32_bf16 %[o], v[116:119], v[64:71], %[ix]\n"
      "ds_read_b128 v[116:119], %[va10] offset:47232\n"
      "v_exp_f32 v76, v76\n"
      "v_exp_f32 v77, v77\n"
      "v_exp_f32 v78, v78\n"
      "v_exp_f32 v79, v79\n"
      "s_waitcnt lgkmcnt(2)\n"
      "v_mfma_f32_32x32x16_bf16 v[96:111], v[112:115], %[qm], %[cm]\n"
      "ds_read_b128 v[112:115], %[ka] offset:53760\n"
      "v_exp_f32 v80, v80\n"
      "v_exp_f32 v81, v81\n"
      "v_exp_f32 v82, v82\n"
      "v_exp_f32 v83, v83\n"
      "v_mfma_f32_32x32x8_bf16 v[96:111], v[124:125], %[qt], v[96:111]\n"
      "ds_read_b64 v[124:125], %[kx1] offset:53760\n"
      "v_exp_f32 v84, v84\n"
      "v_exp_f32 v85, v85\n"
      "v_exp_f32 v86, v86\n"
      "v_exp_f32 v87, v87\n"
      "s_waitcnt lgkmcnt(2)\n"
      "v_smfmac_f32_32x32x32_bf16 %[o], v[120:123], v[72:79], %[ix]\n"
      "ds_read_b128 v[120:123], %[va11] offset:47232\n"
      "v_exp_f32 v88, v88\n"
      "v_exp_f32 v89, v89\n"
      "v_exp_f32 v90, v90\n"
      "v_exp_f32 v91, v91\n"
      "v_smfmac_f32_32x32x32_bf16 %[o], v[116:119], v[80:87], %[ix]\n"
      "ds_read_b128 v[116:119], %[va00] offset:52480\n"
      "v_exp_f32 v92, v92\n"
      "v_exp_f32 v93, v93\n"
      "v_exp_f32 v94, v94\n"
      "v_exp_f32 v95, v95\n"
      "s_waitcnt lgkmcnt(2)\n"
      "v_mfma_f32_32x32x16_bf16 v[64:79], v[112:115], %[qm], %[cm]\n"
      "ds_read_b128 v[112:115], %[ka] offset:57728\n"
      "v_exp_f32 v96, v96\n"
      "v_exp_f32 v97, v97\n"
      "v_exp_f32 v98, v98\n"
      "v_exp_f32 v99, v99\n"
      "v_mfma_f32_32x32x8_bf16 v[64:79], v[124:125], %[qt], v[64:79]\n"
      "ds_read_b64 v[124:125], %[kx0] offset:57728\n"
      "v_exp_f32 v100, v100\n"
      "v_exp_f32 v101, v101\n"
      "v_exp_f32 v102, v102\n"
      "v_exp_f32 v103, v103\n"
      "s_waitcnt lgkmcnt(2)\n"
      "v_smfmac_f32_32x32x32_bf16 %[o], v[120:123], v[88:95], %[ix]\n"
      "ds_read_b128 v[120:123], %[va01] offset:52480\n"
      "v_exp_f32 v104, v104\n"
      "v_exp_f32 v105, v105\n"
      "v_exp_f32 v106, v106\n"
      "v_exp_f32 v107, v107\n"
      "v_smfmac_f32_32x32x32_bf16 %[o], v[116:119], v[96:103], %[ix]\n"
      "ds_read_b128 v[116:119], %[va10] offset:52480\n"
      "v_exp_f32 v108, v108\n"
      "v_exp_f32 v109, v109\n"
      "v_exp_f32 v110, v110\n"
      "v_exp_f32 v111, v111\n"
      "s_waitcnt lgkmcnt(2)\n"
      "v_mfma_f32_32x32x16_bf16 v[80:95], v[112:115], %[qm], %[cm]\n"
      "ds_read_b128 v[112:115], %[ka] offset:59008\n"
      "v_exp_f32 v64, v64\n"
      "v_exp_f32 v65, v65\n"
      "v_exp_f32 v66, v66\n"
      "v_exp_f32 v67, v67\n"
      "v_mfma_f32_32x32x8_bf16 v[80:95], v[124:125], %[qt], v[80:95]\n"
      "ds_read_b64 v[124:125], %[kx1] offset:59008\n"
      "v_exp_f32 v68, v68\n"
      "v_exp_f32 v69, v69\n"
      "v_exp_f32 v70, v70\n"
      "v_exp_f32 v71, v71\n"
      "s_waitcnt lgkmcnt(2)\n"
      "v_smfmac_f32_32x32x32_bf16 %[o], v[120:123], v[104:111], %[ix]\n"
      "ds_read_b128 v[120:123], %[va11] offset:52480\n"
      "v_exp_f32 v72, v72\n"
      "v_exp_f32 v73, v73\n"
      "v_exp_f32 v74, v74\n"
      "v_exp_f32 v75, v75\n"
      "v_smfmac_f32_32x32x32_bf16 %[o], v[116:119], v[64:71], %[ix]\n"
      "ds_read_b128 v[116:119], %[va00] offset:57728\n"
      "v_exp_f32 v76, v76\n"
      "v_exp_f32 v77, v77\n"
      "v_exp_f32 v78, v78\n"
      "v_exp_f32 v79, v79\n"
      "s_waitcnt lgkmcnt(2)\n"
      "v_mfma_f32_32x32x16_bf16 v[96:111], v[112:115], %[qm], %[cm]\n"
      "s_barrier\n"
      "ds_read_b128 v[112:115], %[ka] offset:0\n"
      "v_exp_f32 v80, v80\n"
      "v_exp_f32 v81, v81\n"
      "v_exp_f32 v82, v82\n"
      "v_exp_f32 v83, v83\n"
      "v_mfma_f32_32x32x8_bf16 v[96:111], v[124:125], %[qt], v[96:111]\n"
      "ds_read_b64 v[124:125], %[kx0] offset:0\n"
      "v_exp_f32 v84, v84\n"
      "v_exp_f32 v85, v85\n"
      "v_exp_f32 v86, v86\n"
      "v_exp_f32 v87, v87\n"
      "s_waitcnt lgkmcnt(2)\n"
      "v_smfmac_f32_32x32x32_bf16 %[o], v[120:123], v[72:79], %[ix]\n"
      "ds_read_b128 v[120:123], %[va01] offset:57728\n"
      "v_exp_f32 v88, v88\n"
      "v_exp_f32 v89, v89\n"
      "v_exp_f32 v90, v90\n"
      "v_exp_f32 v91, v91\n"
      "v_smfmac_f32_32x32x32_bf16 %[o], v[116:119], v[80:87], %[ix]\n"
      "ds_read_b128 v[116:119], %[va10] offset:57728\n"
      "v_exp_f32 v92, v92\n"
      "v_exp_f32 v93, v93\n"
      "v_exp_f32 v94, v94\n"
      "v_exp_f32 v95, v95\n"
      "s_waitcnt lgkmcnt(2)\n"
      "v_mfma_f32_32x32x16_bf16 v[64:79], v[112:115], %[qm], %[cm]\n"
      "ds_read_b128 v[112:115], %[ka] offset:1280\n"
      "v_exp_f32 v96, v96\n"
      "v_exp_f32 v97, v97\n"
      "v_exp_f32 v98, v98\n"
      "v_exp_f32 v99, v99\n"
      "v_mfma_f32_32x32x8_bf16 v[64:79], v[124:125], %[qt], v[64:79]\n"
      "ds_read_b64 v[124:125], %[kx1] offset:1280\n"
      "v_exp_f32 v100, v100\n"
      "v_exp_f32 v101, v101\n"
      "v_exp_f32 v102, v102\n"
      "v_exp_f32 v103, v103\n"
      "s_waitcnt lgkmcnt(2)\n"
      "v_smfmac_f32_32x32x32_bf16 %[o], v[120:123], v[88:95], %[ix]\n"
      "ds_read_b128 v[120:123], %[va11] offset:57728\n"
      "v_exp_f32 v104, v104\n"
      "v_exp_f32 v105, v105\n"
      "v_exp_f32 v106, v106\n"
      "v_exp_f32 v107, v107\n"
      "v_smfmac_f32_32x32x32_bf16 %[o], v[116:119], v[96:103], %[ix]\n"
      "ds_read_b128 v[116:119], %[va00] offset:0\n"
      "v_exp_f32 v108, v108\n"
      "v_exp_f32 v109, v109\n"
      "v_exp_f32 v110, v110\n"
      "v_exp_f32 v111, v111\n"
      "s_waitcnt lgkmcnt(2)\n"
      "v_mfma_f32_32x32x16_bf16 v[80:95], v[112:115], %[qm], %[cm]\n"
      "ds_read_b128 v[112:115], %[ka] offset:5248\n"
      "v_exp_f32 v64, v64\n"
      "v_exp_f32 v65, v65\n"
      "v_exp_f32 v66, v66\n"
      "v_exp_f32 v67, v67\n"
      "v_mfma_f32_32x32x8_bf16 v[80:95], v[124:125], %[qt], v[80:95]\n"
      "ds_read_b64 v[124:125], %[kx0] offset:5248\n"
      "v_exp_f32 v68, v68\n"
      "v_exp_f32 v69, v69\n"
      "v_exp_f32 v70, v70\n"
      "v_exp_f32 v71, v71\n"
      "s_waitcnt lgkmcnt(2)\n"
      "v_smfmac_f32_32x32x32_bf16 %[o], v[120:123], v[104:111], %[ix]\n"
      "ds_read_b128 v[120:123], %[va01] offset:0\n"
      "v_exp_f32 v72, v72\n"
      "v_exp_f32 v73, v73\n"
      "v_exp_f32 v74, v74\n"
      "v_exp_f32 v75, v75\n"
      "v_smfmac_f32_32x32x32_bf16 %[o], v[116:119], v[64:71], %[ix]\n"
      "ds_read_b128 v[116:119], %[va10] offset:0\n"
      "v_exp_f32 v76, v76\n"
      "v_exp_f32 v77, v77\n"
      "v_exp_f32 v78, v78\n"
      "v_exp_f32 v79, v79\n"
      "s_waitcnt lgkmcnt(2)\n"
      "v_mfma_f32_32x32x16_bf16 v[96:111], v[112:115], %[qm], %[cm]\n"
      "ds_read_b128 v[112:115], %[ka] offset:6528\n"
      "v_exp_f32 v80, v80\n"
      "v_exp_f32 v81, v81\n"
      "v_exp_f32 v82, v82\n"
      "v_exp_f32 v83, v83\n"
      "v_mfma_f32_32x32x8_bf16 v[96:111], v[124:125], %[qt], v[96:111]\n"
      "ds_read_b64 v[124:125], %[kx1] offset:6528\n"
      "v_exp_f32 v84, v84\n"
      "v_exp_f32 v85, v85\n"
      "v_exp_f32 v86, v86\n"
      "v_exp_f32 v87, v87\n"
      "s_waitcnt lgkmcnt(2)\n"
      "v_smfmac_f32_32x32x32_bf16 %[o], v[120:123], v[72:79], %[ix]\n"
      "ds_read_b128 v[120:123], %[va11] offset:0\n"
      "v_exp_f32 v88, v88\n"
      "v_exp_f32 v89, v89\n"
      "v_exp_f32 v90, v90\n"
      "v_exp_f32 v91, v91\n"
      "v_smfmac_f32_32x32x32_bf16 %[o], v[116:119], v[80:87], %[ix]\n"
      "ds_read_b128 v[116:119], %[va00] offset:5248\n"
      "v_exp_f32 v92, v92\n"
      "v_exp_f32 v93, v93\n"
      "v_exp_f32 v94, v94\n"
      "v_exp_f32 v95, v95\n"
      "s_waitcnt lgkmcnt(2)\n"
      "v_mfma_f32_32x32x16_bf16 v[64:79], v[112:115], %[qm], %[cm]\n"
      "ds_read_b128 v[112:115], %[ka] offset:10496\n"
      "v_exp_f32 v96, v96\n"
      "v_exp_f32 v97, v97\n"
      "v_exp_f32 v98, v98\n"
      "v_exp_f32 v99, v99\n"
      "v_mfma_f32_32x32x8_bf16 v[64:79], v[124:125], %[qt], v[64:79]\n"
      "ds_read_b64 v[124:125], %[kx0] offset:10496\n"
      "v_exp_f32 v100, v100\n"
      "v_exp_f32 v101, v101\n"
      "v_exp_f32 v102, v102\n"
      "v_exp_f32 v103, v103\n"
      "s_waitcnt lgkmcnt(2)\n"
      "v_smfmac_f32_32x32x32_bf16 %[o], v[120:123], v[88:95], %[ix]\n"
      "ds_read_b128 v[120:123], %[va01] offset:5248\n"
      "v_exp_f32 v104, v104\n"
      "v_exp_f32 v105, v105\n"
      "v_exp_f32 v106, v106\n"
      "v_exp_f32 v107, v107\n"
      "v_smfmac_f32_32x32x32_bf16 %[o], v[116:119], v[96:103], %[ix]\n"
      "ds_read_b128 v[116:119], %[va10] offset:5248\n"
      "v_exp_f32 v108, v108\n"
      "v_exp_f32 v109, v109\n"
      "v_exp_f32 v110, v110\n"
      "v_exp_f32 v111, v111\n"
      "s_waitcnt lgkmcnt(2)\n"
      "v_mfma_f32_32x32x16_bf16 v[80:95], v[112:115], %[qm], %[cm]\n"
      "ds_read_b128 v[112:115], %[ka] offset:11776\n"
      "v_exp_f32 v64, v64\n"
      "v_exp_f32 v65, v65\n"
      "v_exp_f32 v66, v66\n"
      "v_exp_f32 v67, v67\n"
      "v_mfma_f32_32x32x8_bf16 v[80:95], v[124:125], %[qt], v[80:95]\n"
      "ds_read_b64 v[124:125], %[kx1] offset:11776\n"
      "v_exp_f32 v68, v68\n"
      "v_exp_f32 v69, v69\n"
      "v_exp_f32 v70, v70\n"
      "v_exp_f32 v71, v71\n"
      "s_waitcnt lgkmcnt(2)\n"
      "v_smfmac_f32_32x32x32_bf16 %[o], v[120:123], v[104:111], %[ix]\n"
      "ds_read_b128 v[120:123], %[va11] offset:5248\n"
      "v_exp_f32 v72, v72\n"
      "v_exp_f32 v73, v73\n"
      "v_exp_f32 v74, v74\n"
      "v_exp_f32 v75, v75\n"
      "v_smfmac_f32_32x32x32_bf16 %[o], v[116:119], v[64:71], %[ix]\n"
      "ds_read_b128 v[116:119], %[va00] offset:10496\n"
      "v_exp_f32 v76, v76\n"
      "v_exp_f32 v77, v77\n"
      "v_exp_f32 v78, v78\n"
      "v_exp_f32 v79, v79\n"
      "s_waitcnt lgkmcnt(2)\n"
      "v_mfma_f32_32x32x16_bf16 v[96:111], v[112:115], %[qm], %[cm]\n"
      "ds_read_b128 v[112:115], %[ka] offset:15744\n"
      "v_exp_f32 v80, v80\n"
      "v_exp_f32 v81, v81\n"
      "v_exp_f32 v82, v82\n"
      "v_exp_f32 v83, v83\n"
      "v_mfma_f32_32x32x8_bf16 v[96:111], v[124:125], %[qt], v[96:111]\n"
      "ds_read_b64 v[124:125], %[kx0] offset:15744\n"
      "v_exp_f32 v84, v84\n"
      "v_exp_f32 v85, v85\n"
      "v_exp_f32 v86, v86\n"
      "v_exp_f32 v87, v87\n"
      "s_waitcnt lgkmcnt(2)\n"
      "v_smfmac_f32_32x32x32_bf16 %[o], v[120:123], v[72:79], %[ix]\n"
      "ds_read_b128 v[120:123], %[va01] offset:10496\n"
      "v_exp_f32 v88, v88\n"
      "v_exp_f32 v89, v89\n"
      "v_exp_f32 v90, v90\n"
      "v_exp_f32 v91, v91\n"
      "v_smfmac_f32_32x32x32_bf16 %[o], v[116:119], v[80:87], %[ix]\n"
      "ds_read_b128 v[116:119], %[va10] offset:10496\n"
      "v_exp_f32 v92, v92\n"
      "v_exp_f32 v93, v93\n"
      "v_exp_f32 v94, v94\n"
      "v_exp_f32 v95, v95\n"
      "s_waitcnt lgkmcnt(2)\n"
      "v_mfma_f32_32x32x16_bf16 v[64:79], v[112:115], %[qm], %[cm]\n"
      "ds_read_b128 v[112:115], %[ka] offset:17024\n"
      "v_exp_f32 v96, v96\n"
      "v_exp_f32 v97, v97\n"
      "v_exp_f32 v98, v98\n"
      "v_exp_f32 v99, v99\n"
      "v_mfma_f32_32x32x8_bf16 v[64:79], v[124:125], %[qt], v[64:79]\n"
      "ds_read_b64 v[124:125], %[kx1] offset:17024\n"
      "v_exp_f32 v100, v100\n"
      "v_exp_f32 v101, v101\n"
      "v_exp_f32 v102, v102\n"
      "v_exp_f32 v103, v103\n"
      "s_waitcnt lgkmcnt(2)\n"
      "v_smfmac_f32_32x32x32_bf16 %[o], v[120:123], v[88:95], %[ix]\n"
      "ds_read_b128 v[120:123], %[va11] offset:10496\n"
      "v_exp_f32 v104, v104\n"
      "v_exp_f32 v105, v105\n"
      "v_exp_f32 v106, v106\n"
      "v_exp_f32 v107, v107\n"
      "v_smfmac_f32_32x32x32_bf16 %[o], v[116:119], v[96:103], %[ix]\n"
      "ds_read_b128 v[116:119], %[va00] offset:15744\n"
      "v_exp_f32 v108, v108\n"
      "v_exp_f32 v109, v109\n"
      "v_exp_f32 v110, v110\n"
      "v_exp_f32 v111, v111\n"
      "s_waitcnt lgkmcnt(2)\n"
      "v_mfma_f32_32x32x16_bf16 v[80:95], v[112:115], %[qm], %[cm]\n"
      "s_barrier\n"
      "ds_read_b128 v[112:115], %[ka] offset:20992\n"
      "v_exp_f32 v64, v64\n"
      "v_exp_f32 v65, v65\n"
      "v_exp_f32 v66, v66\n"
      "v_exp_f32 v67, v67\n"
      "v_mfma_f32_32x32x8_bf16 v[80:95], v[124:125], %[qt], v[80:95]\n"
      "ds_read_b64 v[124:125], %[kx0] offset:20992\n"
      "v_exp_f32 v68, v68\n"
      "v_exp_f32 v69, v69\n"
      "v_exp_f32 v70, v70\n"
      "v_exp_f32 v71, v71\n"
      "s_waitcnt lgkmcnt(2)\n"
      "v_smfmac_f32_32x32x32_bf16 %[o], v[120:123], v[104:111], %[ix]\n"
      "ds_read_b128 v[120:123], %[va01] offset:15744\n"
      "v_exp_f32 v72, v72\n"
      "v_exp_f32 v73, v73\n"
      "v_exp_f32 v74, v74\n"
      "v_exp_f32 v75, v75\n"
      "v_smfmac_f32_32x32x32_bf16 %[o], v[116:119], v[64:71], %[ix]\n"
      "ds_read_b128 v[116:119], %[va10] offset:15744\n"
      "v_exp_f32 v76, v76\n"
      "v_exp_f32 v77, v77\n"
      "v_exp_f32 v78, v78\n"
      "v_exp_f32 v79, v79\n"
      "s_waitcnt lgkmcnt(2)\n"
      "v_mfma_f32_32x32x16_bf16 v[96:111], v[112:115], %[qm], %[cm]\n"
      "ds_read_b128 v[112:115], %[ka] offset:22272\n"
      "v_exp_f32 v80, v80\n"
      "v_exp_f32 v81, v81\n"
      "v_exp_f32 v82, v82\n"
      "v_exp_f32 v83, v83\n"
      "v_mfma_f32_32x32x8_bf16 v[96:111], v[124:125], %[qt], v[96:111]\n"
      "ds_read_b64 v[124:125], %[kx1] offset:22272\n"
      "v_exp_f32 v84, v84\n"
      "v_exp_f32 v85, v85\n"
      "v_exp_f32 v86, v86\n"
      "v_exp_f32 v87, v87\n"
      "s_waitcnt lgkmcnt(2)\n"
      "v_smfmac_f32_32x32x32_bf16 %[o], v[120:123], v[72:79], %[ix]\n"
      "ds_read_b128 v[120:123], %[va11] offset:15744\n"
      "v_exp_f32 v88, v88\n"
      "v_exp_f32 v89, v89\n"
      "v_exp_f32 v90, v90\n"
      "v_exp_f32 v91, v91\n"
      "v_smfmac_f32_32x32x32_bf16 %[o], v[116:119], v[80:87], %[ix]\n"
      "ds_read_b128 v[116:119], %[va00] offset:20992\n"
      "v_exp_f32 v92, v92\n"
      "v_exp_f32 v93, v93\n"
      "v_exp_f32 v94, v94\n"
      "v_exp_f32 v95, v95\n"
      "s_waitcnt lgkmcnt(2)\n"
      "v_mfma_f32_32x32x16_bf16 v[64:79], v[112:115], %[qm], %[cm]\n"
      "ds_read_b128 v[112:115], %[ka] offset:26240\n"
      "v_exp_f32 v96, v96\n"
      "v_exp_f32 v97, v97\n"
      "v_exp_f32 v98, v98\n"
      "v_exp_f32 v99, v99\n"
      "v_mfma_f32_32x32x8_bf16 v[64:79], v[124:125], %[qt], v[64:79]\n"
      "ds_read_b64 v[124:125], %[kx0] offset:26240\n"
      "v_exp_f32 v100, v100\n"
      "v_exp_f32 v101, v101\n"
      "v_exp_f32 v102, v102\n"
      "v_exp_f32 v103, v103\n"
      "s_waitcnt lgkmcnt(2)\n"
      "v_smfmac_f32_32x32x32_bf16 %[o], v[120:123], v[88:95], %[ix]\n"
      "ds_read_b128 v[120:123], %[va01] offset:20992\n"
      "v_exp_f32 v104, v104\n"
      "v_exp_f32 v105, v105\n"
      "v_exp_f32 v106, v106\n"
      "v_exp_f32 v107, v107\n"
      "v_smfmac_f32_32x32x32_bf16 %[o], v[116:119], v[96:103], %[ix]\n"
      "ds_read_b128 v[116:119], %[va10] offset:20992\n"
      "v_exp_f32 v108, v108\n"
      "v_exp_f32 v109, v109\n"
      "v_exp_f32 v110, v110\n"
      "v_exp_f32 v111, v111\n"
      "s_waitcnt lgkmcnt(2)\n"
      "v_mfma_f32_32x32x16_bf16 v[80:95], v[112:115], %[qm], %[cm]\n"
      "ds_read_b128 v[112:115], %[ka] offset:27520\n"
      "v_exp_f32 v64, v64\n"
      "v_exp_f32 v65, v65\n"
      "v_exp_f32 v66, v66\n"
      "v_exp_f32 v67, v67\n"
      "v_mfma_f32_32x32x8_bf16 v[80:95], v[124:125], %[qt], v[80:95]\n"
      "ds_read_b64 v[124:125], %[kx1] offset:27520\n"
      "v_exp_f32 v68, v68\n"
      "v_exp_f32 v69, v69\n"
      "v_exp_f32 v70, v70\n"
      "v_exp_f32 v71, v71\n"
      "s_waitcnt lgkmcnt(2)\n"
      "v_smfmac_f32_32x32x32_bf16 %[o], v[120:123], v[104:111], %[ix]\n"
      "ds_read_b128 v[120:123], %[va11] offset:20992\n"
      "v_exp_f32 v72, v72\n"
      "v_exp_f32 v73, v73\n"
      "v_exp_f32 v74, v74\n"
      "v_exp_f32 v75, v75\n"
      "v_smfmac_f32_32x32x32_bf16 %[o], v[116:119], v[64:71], %[ix]\n"
      "ds_read_b128 v[116:119], %[va00] offset:26240\n"
      "v_exp_f32 v76, v76\n"
      "v_exp_f32 v77, v77\n"
      "v_exp_f32 v78, v78\n"
      "v_exp_f32 v79, v79\n"
      "s_waitcnt lgkmcnt(2)\n"
      "v_mfma_f32_32x32x16_bf16 v[96:111], v[112:115], %[qm], %[cm]\n"
      "ds_read_b128 v[112:115], %[ka] offset:31488\n"
      "v_exp_f32 v80, v80\n"
      "v_exp_f32 v81, v81\n"
      "v_exp_f32 v82, v82\n"
      "v_exp_f32 v83, v83\n"
      "v_mfma_f32_32x32x8_bf16 v[96:111], v[124:125], %[qt], v[96:111]\n"
      "ds_read_b64 v[124:125], %[kx0] offset:31488\n"
      "v_exp_f32 v84, v84\n"
      "v_exp_f32 v85, v85\n"
      "v_exp_f32 v86, v86\n"
      "v_exp_f32 v87, v87\n"
      "s_waitcnt lgkmcnt(2)\n"
      "v_smfmac_f32_32x32x32_bf16 %[o], v[120:123], v[72:79], %[ix]\n"
      "ds_read_b128 v[120:123], %[va01] offset:26240\n"
      "v_exp_f32 v88, v88\n"
      "v_exp_f32 v89, v89\n"
      "v_exp_f32 v90, v90\n"
      "v_exp_f32 v91, v91\n"
      "v_smfmac_f32_32x32x32_bf16 %[o], v[116:119], v[80:87], %[ix]\n"
      "ds_read_b128 v[116:119], %[va10] offset:26240\n"
      "v_exp_f32 v92, v92\n"
      "v_exp_f32 v93, v93\n"
      "v_exp_f32 v94, v94\n"
      "v_exp_f32 v95, v95\n"
      "s_waitcnt lgkmcnt(2)\n"
      "v_mfma_f32_32x32x16_bf16 v[64:79], v[112:115], %[qm], %[cm]\n"
      "ds_read_b128 v[112:115], %[ka] offset:32768\n"
      "v_exp_f32 v96, v96\n"
      "v_exp_f32 v97, v97\n"
      "v_exp_f32 v98, v98\n"
      "v_exp_f32 v99, v99\n"
      "v_mfma_f32_32x32x8_bf16 v[64:79], v[124:125], %[qt], v[64:79]\n"
      "ds_read_b64 v[124:125], %[kx1] offset:32768\n"
      "v_exp_f32 v100, v100\n"
      "v_exp_f32 v101, v101\n"
      "v_exp_f32 v102, v102\n"
      "v_exp_f32 v103, v103\n"
      "s_waitcnt lgkmcnt(2)\n"
      "v_smfmac_f32_32x32x32_bf16 %[o], v[120:123], v[88:95], %[ix]\n"
      "ds_read_b128 v[120:123], %[va11] offset:26240\n"
      "v_exp_f32 v104, v104\n"
      "v_exp_f32 v105, v105\n"
      "v_exp_f32 v106, v106\n"
      "v_exp_f32 v107, v107\n"
      "v_smfmac_f32_32x32x32_bf16 %[o], v[116:119], v[96:103], %[ix]\n"
      "ds_read_b128 v[116:119], %[va00] offset:31488\n"
      "v_exp_f32 v108, v108\n"
      "v_exp_f32 v109, v109\n"
      "v_exp_f32 v110, v110\n"
      "v_exp_f32 v111, v111\n"
      "s_waitcnt lgkmcnt(2)\n"
      "v_mfma_f32_32x32x16_bf16 v[80:95], v[112:115], %[qm], %[cm]\n"
      "ds_read_b128 v[112:115], %[ka] offset:36736\n"
      "v_exp_f32 v64, v64\n"
      "v_exp_f32 v65, v65\n"
      "v_exp_f32 v66, v66\n"
      "v_exp_f32 v67, v67\n"
      "v_mfma_f32_32x32x8_bf16 v[80:95], v[124:125], %[qt], v[80:95]\n"
      "ds_read_b64 v[124:125], %[kx0] offset:36736\n"
      "v_exp_f32 v68, v68\n"
      "v_exp_f32 v69, v69\n"
      "v_exp_f32 v70, v70\n"
      "v_exp_f32 v71, v71\n"
      "s_waitcnt lgkmcnt(2)\n"
      "v_smfmac_f32_32x32x32_bf16 %[o], v[120:123], v[104:111], %[ix]\n"
      "ds_read_b128 v[120:123], %[va01] offset:31488\n"
      "v_exp_f32 v72, v72\n"
      "v_exp_f32 v73, v73\n"
      "v_exp_f32 v74, v74\n"
      "v_exp_f32 v75, v75\n"
      "v_smfmac_f32_32x32x32_bf16 %[o], v[116:119], v[64:71], %[ix]\n"
      "ds_read_b128 v[116:119], %[va10] offset:31488\n"
      "v_exp_f32 v76, v76\n"
      "v_exp_f32 v77, v77\n"
      "v_exp_f32 v78, v78\n"
      "v_exp_f32 v79, v79\n"
      "s_waitcnt lgkmcnt(2)\n"
      "v_mfma_f32_32x32x16_bf16 v[96:111], v[112:115], %[qm], %[cm]\n"
      "ds_read_b128 v[112:115], %[ka] offset:38016\n"
      "v_exp_f32 v80, v80\n"
      "v_exp_f32 v81, v81\n"
      "v_exp_f32 v82, v82\n"
      "v_exp_f32 v83, v83\n"
      "v_mfma_f32_32x32x8_bf16 v[96:111], v[124:125], %[qt], v[96:111]\n"
      "ds_read_b64 v[124:125], %[kx1] offset:38016\n"
      "v_exp_f32 v84, v84\n"
      "v_exp_f32 v85, v85\n"
      "v_exp_f32 v86, v86\n"
      "v_exp_f32 v87, v87\n"
      "s_waitcnt lgkmcnt(2)\n"
      "v_smfmac_f32_32x32x32_bf16 %[o], v[120:123], v[72:79], %[ix]\n"
      "ds_read_b128 v[120:123], %[va11] offset:31488\n"
      "v_exp_f32 v88, v88\n"
      "v_exp_f32 v89, v89\n"
      "v_exp_f32 v90, v90\n"
      "v_exp_f32 v91, v91\n"
      "v_smfmac_f32_32x32x32_bf16 %[o], v[116:119], v[80:87], %[ix]\n"
      "ds_read_b128 v[116:119], %[va00] offset:36736\n"
      "v_exp_f32 v92, v92\n"
      "v_exp_f32 v93, v93\n"
      "v_exp_f32 v94, v94\n"
      "v_exp_f32 v95, v95\n"
      "s_waitcnt lgkmcnt(2)\n"
      "v_mfma_f32_32x32x16_bf16 v[64:79], v[112:115], %[qm], %[cm]\n"
      "s_barrier\n"
      "ds_read_b128 v[112:115], %[ka] offset:41984\n"
      "v_exp_f32 v96, v96\n"
      "v_exp_f32 v97, v97\n"
      "v_exp_f32 v98, v98\n"
      "v_exp_f32 v99, v99\n"
      "v_mfma_f32_32x32x8_bf16 v[64:79], v[124:125], %[qt], v[64:79]\n"
      "ds_read_b64 v[124:125], %[kx0] offset:41984\n"
      "v_exp_f32 v100, v100\n"
      "v_exp_f32 v101, v101\n"
      "v_exp_f32 v102, v102\n"
      "v_exp_f32 v103, v103\n"
      "s_waitcnt lgkmcnt(2)\n"
      "v_smfmac_f32_32x32x32_bf16 %[o], v[120:123], v[88:95], %[ix]\n"
      "ds_read_b128 v[120:123], %[va01] offset:36736\n"
      "v_exp_f32 v104, v104\n"
      "v_exp_f32 v105, v105\n"
      "v_exp_f32 v106, v106\n"
      "v_exp_f32 v107, v107\n"
      "v_smfmac_f32_32x32x32_bf16 %[o], v[116:119], v[96:103], %[ix]\n"
      "ds_read_b128 v[116:119], %[va10] offset:36736\n"
      "v_exp_f32 v108, v108\n"
      "v_exp_f32 v109, v109\n"
      "v_exp_f32 v110, v110\n"
      "v_exp_f32 v111, v111\n"
      "s_waitcnt lgkmcnt(2)\n"
      "v_mfma_f32_32x32x16_bf16 v[80:95], v[112:115], %[qm], %[cm]\n"
      "ds_read_b128 v[112:115], %[ka] offset:43264\n"
      "v_exp_f32 v64, v64\n"
      "v_exp_f32 v65, v65\n"
      "v_exp_f32 v66, v66\n"
      "v_exp_f32 v67, v67\n"
      "v_mfma_f32_32x32x8_bf16 v[80:95], v[124:125], %[qt], v[80:95]\n"
      "ds_read_b64 v[124:125], %[kx1] offset:43264\n"
      "v_exp_f32 v68, v68\n"
      "v_exp_f32 v69, v69\n"
      "v_exp_f32 v70, v70\n"
      "v_exp_f32 v71, v71\n"
      "s_waitcnt lgkmcnt(2)\n"
      "v_smfmac_f32_32x32x32_bf16 %[o], v[120:123], v[104:111], %[ix]\n"
      "ds_read_b128 v[120:123], %[va11] offset:36736\n"
      "v_exp_f32 v72, v72\n"
      "v_exp_f32 v73, v73\n"
      "v_exp_f32 v74, v74\n"
      "v_exp_f32 v75, v75\n"
      "v_smfmac_f32_32x32x32_bf16 %[o], v[116:119], v[64:71], %[ix]\n"
      "ds_read_b128 v[116:119], %[va00] offset:41984\n"
      "v_exp_f32 v76, v76\n"
      "v_exp_f32 v77, v77\n"
      "v_exp_f32 v78, v78\n"
      "v_exp_f32 v79, v79\n"
      "s_sub_u32 %[it], %[it], 1\n"
      "s_cmp_lg_u32 %[it], 0\n"
      "s_cbranch_scc1 1b\n"
      "s_waitcnt lgkmcnt(0)\n"
      "s_nop 1\n"
      "v_smfmac_f32_32x32x32_bf16 %[o], v[120:123], v[72:79], %[ix]\n"
      "s_nop 15\n"
      : [o] "+v"(o), "+{v[64:79]}"(s), [it] "=&s"(it)
      : [qm] "v"(qm), [qt] "v"(qt), [cm] "v"(cm), [ka] "v"(sm + AT_PAD + ko), [kx0] "v"(sm + AT_PAD + xo),
        [kx1] "v"(sm + AT_PAD + xo),
        [va00] "v"(va[0][0]), [va01] "v"(va[0][1]), [va10] "v"(va[1][0]), [va11] "v"(va[1][1]), [ix] "v"(0xdddd),
        [d0] "v"(dv[0]), [d1] "v"(dv[1]), [d2] "v"(dv[2]), [d3] "v"(dv[3]), [d4] "v"(dv[4]), [d5] "v"(dv[5]), [a0] "s"(da[0]), [a1] "s"(da[1]),
        [a2] "s"(da[2]), [a3] "s"(da[3]), [a4] "s"(da[4]), [a5] "s"(da[5]), [ns] "s"(ns), [rs] "s"(rs)
      : "v80", "v81", "v82", "v83", "v84", "v85", "v86", "v87", "v88", "v89", "v90", "v91", "v92", "v93", "v94", "v95", "v96", "v97", "v98", "v99", "v100", "v101", "v102", "v103", "v104", "v105", "v106", "v107", "v108", "v109", "v110", "v111", "v112", "v113", "v114", "v115", "v116", "v117", "v118", "v119", "v120", "v121", "v122", "v123", "v124", "v125", "scc", "memory");
  }
  float a = 0.f;
#pragma unroll
  for (int i = 0; i < 8; ++i) a += fabsf(o[i]);
  if (hf == 0) {
#pragma unroll
    for (int i = 8; i < 12; ++i) a += fabsf(o[i]);
  }
  const bool bad = !(a < 3.0e38f) || (hf && !(o[8] < 1.8e19f));
  if (__builtin_amdgcn_ballot_w64(bad)) {
    __builtin_amdgcn_fence(__ATOMIC_ACQUIRE, "agent");
    const f32x16 zero = {};
    float m = -INFINITY;
    for (int t = 0; t < 64; ++t) {
      const char* p = kvh + (t >> 1) * KV_PAIR + (t & 1) * 1280;
      m = fmaxf(m, max16(qk(*(const bf16x8*)(p + ko), *(const s16x4*)(p + xo), qm, qt, zero)));
    }
    m = halfmax(m);
    o = zero;
    for (int t = 0; t < 64; ++t) {
      const char* p = kvh + (t >> 1) * KV_PAIR + (t & 1) * 1280;
      const char* pv = kvh + (t >> 1) * KV_PAIR + (t & 1) * 3840;
      const bf16x8 one = __builtin_bit_cast(bf16x8, u32x4{0x3f803f80u, 0x3f803f80u, 0x3f803f80u, 0x3f803f80u});
      const bf16x8 v0 = *(const bf16x8*)(pv + vo[0]), v1 = *(const bf16x8*)(pv + vo[1]);
      f32x16 s = qk(*(const bf16x8*)(p + ko), *(const s16x4*)(p + xo), qm, qt, zero);
#pragma unroll
      for (int i = 0; i < 16; ++i) s[i] = __builtin_amdgcn_exp2f(s[i] - m);
      f32x8 lo = __builtin_shufflevector(s, s, 0, 1, 2, 3, 4, 5, 6, 7), hi = __builtin_shufflevector(s, s, 8, 9, 10, 11, 12, 13, 14, 15);
      o = __builtin_amdgcn_smfmac_f32_32x32x32_bf16(ones ? one : v0, __builtin_bit_cast(bf16x16, lo), o, 0xdddd, 0, 0);
      o = __builtin_amdgcn_smfmac_f32_32x32x32_bf16(ones ? one : v1, __builtin_bit_cast(bf16x16, hi), o, 0xdddd, 0, 0);
    }
  }
  {
    auto r = __builtin_amdgcn_permlane32_swap(__float_as_uint(o[8]), __float_as_uint(o[8]), false, false);
    const float inv = gq * __builtin_amdgcn_rcpf(__uint_as_float(r[1]));
    auto pk = [&](int i) -> u32 { return __builtin_elementwise_min(pk2(o[i] * inv, o[i + 1] * inv), 0xfffffffeu); };
    const u32 dst = WS_O + (qt0 * 40 + (head >> 3) * 10) * 1024;
    __builtin_amdgcn_raw_buffer_store_b128(u32x4{pk(0), pk(2), pk(4), pk(6)}, rs, lane * 16, dst + (head & 7) * 1024, 16);
    if (!hf)
      __builtin_amdgcn_raw_buffer_store_b64(u32x2{pk(8), pk(10)}, rs, l32 * 16,
                                            dst + (8 + ((head >> 2) & 1)) * 1024 + ((head >> 1) & 1) * 512 + (head & 1) * 8, 16);
  }
  bar();
  if (wid == 0) {
    const rsrc_t ro = mkrsrc(out + (size_t)((b & 7) * 8 + (b >> 5)) * 32 * DM + ((b >> 3) & 3) * 160, 31 * DM * 2 + 320);
    bar();
    finish(sm, 4, lane, ro);
  }
}

extern "C" void kernel_launch(void* const* d_in, const int* in_sizes, int n_in, void* d_out, int out_size,
                              void* d_ws, size_t ws_size, hipStream_t stream) {
  const u16* x = (const u16*)d_in[0];
  const u16* wqkv = (const u16*)d_in[1];
  const u16* wo = (const u16*)d_in[2];
  const u16* w1 = (const u16*)d_in[4];
  const u16* b1 = (const u16*)d_in[5];
  const u16* w2 = (const u16*)d_in[6];
  const u16* b2 = (const u16*)d_in[7];
  const u16* gw = (const u16*)d_in[8];
  char* ws = (char*)d_ws;
  fused<<<256, 768, 0, stream>>>(x, wqkv, w1, b1, gw, w2, wo, b2, ws, (u16*)d_out);
}
```
